```python
import torch

def _softmax(x, axis=-1):
    return torch.softmax(x, dim=axis)
def _gelu(x):
    return torch.nn.functional.gelu(x, approximate="tanh")
def _silu(x):
    return torch.nn.functional.silu(x)
def _sigmoid(x):
    return torch.sigmoid(x)
def _layernorm(x, axis=-1, eps=1e-5):
    m = x.mean(dim=axis, keepdim=True)
    v = x.var(dim=axis, keepdim=True, unbiased=False)
    return (x - m) / torch.sqrt(v + eps)
def _scatter_add(tgt, idx, src, axis=0):
    return tgt.clone().index_add_(axis, idx, src)
def _segment_sum(src, seg, n):
    out = torch.zeros((n,) + src.shape[1:], dtype=src.dtype)
    return out.index_add_(0, seg, src)
def _pad(x, pw):
    flat = tuple(v for pair in reversed(pw) for v in pair)
    return torch.nn.functional.pad(x, flat)

kernel_name = 'recurrent_scan__spectral_gfnet__swiglu_ffn__prompt'

def setup_inputs():

    _w = (lambda a: (lambda t: t.to(torch.bfloat16) if t.is_floating_point() else t)(torch.as_tensor(a)))
    def _A_setup():
        B, S, D = 1, 2048, 512
        Df = D // 2 + 1
        return {
            "x": _w((lambda *s: torch.randn(*s, dtype=torch.bfloat16) * 0.05)(B, S, D)),
            "w_r": _w((lambda *s: torch.randn(*s, dtype=torch.bfloat16) * 0.1)(S, Df) * 0.02),
            "w_i": _w((lambda *s: torch.randn(*s, dtype=torch.bfloat16) * 0.1)(S, Df) * 0.02),
        }
    _A_in = _A_setup()

    def _B_setup():
        B, S, D, F = 1, 2048, 512, 1792
        return {
            "x": _w((lambda *s: torch.randn(*s, dtype=torch.bfloat16) * 0.05)(B, S, D)),
            "w_gate": _w((lambda *s: torch.randn(*s, dtype=torch.bfloat16) * 0.05)(D, F)),
            "w_up": _w((lambda *s: torch.randn(*s, dtype=torch.bfloat16) * 0.05)(D, F)),
            "w_down": _w((lambda *s: torch.randn(*s, dtype=torch.bfloat16) * 0.05)(F, D)),
        }
    _B_in = _B_setup()

    return {
        "A_x": _A_in["x"],
        "A_w_r": _A_in["w_r"],
        "A_w_i": _A_in["w_i"],
        "B_x": _B_in["x"],
        "B_w_gate": _B_in["w_gate"],
        "B_w_up": _B_in["w_up"],
        "B_w_down": _B_in["w_down"],
    }

def reference(A_x, A_w_r, A_w_i, B_x, B_w_gate, B_w_up, B_w_down):
    def _A_ref(x, w_r, w_i):
        B, S, D = x.shape
        X = (lambda x, axes=(-2,-1): torch.fft.rfft2(x.float(), dim=axes))(x, axes=(-2, -1))
        W = w_r + 1j * w_i
        X = X * W
        y = (lambda x, s, axes=(-2,-1): torch.fft.irfft2(x, s=s, dim=axes).to(torch.bfloat16))(X, (S, D), axes=(-2, -1))
        return y

    def _B_ref(x, w_gate, w_up, w_down):
        g = _silu(x @ w_gate)
        u = x @ w_up
        return (g * u) @ w_down

    x = A_x
    xn = _layernorm(x, axis=-1)
    s = _A_ref(xn, A_w_r, A_w_i)
    g = _silu(_B_ref(xn, B_w_gate, B_w_up, B_w_down))
    return x + s * g
```

```cpp
#include <hip/hip_runtime.h>
#include <hip/hip_bf16.h>
#include <stdint.h>

namespace {

typedef __attribute__((ext_vector_type(8))) short bf16x8;
typedef __attribute__((ext_vector_type(4))) short s16x4;
typedef __attribute__((ext_vector_type(4))) float f32x4;
typedef __attribute__((ext_vector_type(4))) unsigned u32x4;
typedef __attribute__((ext_vector_type(2))) unsigned u32x2;

constexpr int SEQ = 2048, DM = 512, NF = 257, FF = 1792;

__device__ const float TWF[4096] = {
1.0f,0.0f, 0.999995291f,-0.00306795677f, 0.999981165f,-0.00613588467f, 0.999957621f,-0.00920375437f,
0.999924719f,-0.0122715384f, 0.99988234f,-0.015339206f, 0.999830604f,-0.0184067301f, 0.99976939f,-0.0214740802f,
0.999698818f,-0.024541229f, 0.999618828f,-0.027608145f, 0.999529421f,-0.030674804f, 0.999430597f,-0.0337411724f,
0.999322355f,-0.0368072242f, 0.999204755f,-0.0398729257f, 0.999077737f,-0.0429382585f, 0.998941302f,-0.0460031815f,
0.99879545f,-0.0490676761f, 0.998640239f,-0.052131705f, 0.998475552f,-0.0551952459f, 0.998301566f,-0.0582582653f,
0.998118103f,-0.061320737f, 0.997925282f,-0.0643826276f, 0.997723043f,-0.0674439222f, 0.997511446f,-0.070504576f,
0.997290432f,-0.0735645667f, 0.997060061f,-0.0766238645f, 0.996820271f,-0.0796824396f, 0.996571124f,-0.0827402622f,
0.996312618f,-0.0857973099f, 0.996044695f,-0.0888535529f, 0.995767415f,-0.0919089541f, 0.995480776f,-0.0949634984f,
0.99518472f,-0.0980171412f, 0.994879305f,-0.10106986f, 0.994564593f,-0.104121633f, 0.994240463f,-0.107172422f,
0.993906975f,-0.110222206f, 0.993564129f,-0.113270953f, 0.993211925f,-0.116318628f, 0.992850423f,-0.119365215f,
0.992479563f,-0.122410677f, 0.992099285f,-0.125454977f, 0.991709769f,-0.128498107f, 0.991310835f,-0.13154003f,
0.990902662f,-0.134580702f, 0.990485072f,-0.137620121f, 0.990058184f,-0.140658244f, 0.989621997f,-0.143695027f,
0.989176512f,-0.146730468f, 0.988721669f,-0.149764538f, 0.988257587f,-0.152797192f, 0.987784147f,-0.155828401f,
0.987301409f,-0.15885815f, 0.986809373f,-0.161886394f, 0.986308098f,-0.164913118f, 0.985797524f,-0.167938292f,
0.985277653f,-0.170961887f, 0.984748483f,-0.173983872f, 0.984210074f,-0.177004218f, 0.983662426f,-0.180022895f,
0.983105481f,-0.183039889f, 0.982539296f,-0.186055154f, 0.981963873f,-0.18906866f, 0.981379211f,-0.192080393f,
0.980785251f,-0.195090324f, 0.980182111f,-0.198098406f, 0.979569793f,-0.201104641f, 0.978948176f,-0.204108968f,
0.97831738f,-0.207111374f, 0.977677345f,-0.210111842f, 0.977028131f,-0.213110313f, 0.976369739f,-0.216106802f,
0.975702107f,-0.219101235f, 0.975025356f,-0.222093627f, 0.974339366f,-0.225083917f, 0.973644257f,-0.228072077f,
0.972939968f,-0.231058106f, 0.972226501f,-0.234041959f, 0.971503913f,-0.237023607f, 0.970772147f,-0.24000302f,
0.970031261f,-0.242980182f, 0.969281256f,-0.24595505f, 0.968522072f,-0.248927608f, 0.967753828f,-0.251897812f,
0.966976464f,-0.254865646f, 0.966189981f,-0.257831097f, 0.965394437f,-0.260794103f, 0.964589775f,-0.263754666f,
0.963776052f,-0.266712755f, 0.962953269f,-0.269668311f, 0.962121427f,-0.272621363f, 0.961280465f,-0.275571823f,
0.960430503f,-0.27851969f, 0.95957154f,-0.281464934f, 0.958703458f,-0.284407526f, 0.957826436f,-0.287347466f,
0.956940353f,-0.290284663f, 0.95604527f,-0.293219149f, 0.955141187f,-0.296150893f, 0.954228103f,-0.299079835f,
0.953306019f,-0.302005947f, 0.952374995f,-0.304929227f, 0.95143503f,-0.307849646f, 0.950486064f,-0.310767144f,
0.949528158f,-0.313681751f, 0.94856137f,-0.316593379f, 0.947585583f,-0.319502026f, 0.946600914f,-0.322407693f,
0.945607305f,-0.32531029f, 0.944604814f,-0.328209847f, 0.943593442f,-0.331106305f, 0.94257319f,-0.333999664f,
0.941544056f,-0.336889863f, 0.940506041f,-0.339776874f, 0.939459205f,-0.342660725f, 0.938403547f,-0.345541328f,
0.937339008f,-0.348418683f, 0.936265647f,-0.351292759f, 0.935183525f,-0.354163527f, 0.934092522f,-0.357030958f,
0.932992816f,-0.359895051f, 0.931884289f,-0.362755716f, 0.93076694f,-0.365612984f, 0.929640889f,-0.368466824f,
0.928506076f,-0.371317208f, 0.927362502f,-0.374164075f, 0.926210225f,-0.377007425f, 0.925049245f,-0.379847199f,
0.923879504f,-0.382683426f, 0.92270112f,-0.385516047f, 0.921514034f,-0.388345033f, 0.920318305f,-0.391170382f,
0.919113874f,-0.393992037f, 0.917900801f,-0.396809995f, 0.916679084f,-0.399624199f, 0.915448725f,-0.402434647f,
0.914209783f,-0.405241311f, 0.912962198f,-0.408044159f, 0.91170603f,-0.410843164f, 0.910441279f,-0.413638324f,
0.909168005f,-0.416429549f, 0.907886088f,-0.419216901f, 0.906595707f,-0.422000259f, 0.905296743f,-0.424779683f,
0.903989315f,-0.427555084f, 0.902673304f,-0.430326492f, 0.901348829f,-0.433093816f, 0.900015891f,-0.435857087f,
0.898674488f,-0.438616246f, 0.897324562f,-0.441371262f, 0.895966232f,-0.444122136f, 0.894599497f,-0.446868837f,
0.893224299f,-0.449611336f, 0.891840696f,-0.452349573f, 0.890448749f,-0.455083579f, 0.889048338f,-0.457813293f,
0.887639642f,-0.460538715f, 0.886222541f,-0.463259786f, 0.884797096f,-0.465976506f, 0.883363366f,-0.468688816f,
0.881921291f,-0.471396744f, 0.880470872f,-0.474100202f, 0.879012227f,-0.47679922f, 0.877545297f,-0.479493767f,
0.876070082f,-0.482183784f, 0.874586642f,-0.484869242f, 0.873094976f,-0.487550169f, 0.871595085f,-0.490226477f,
0.870086968f,-0.492898196f, 0.868570685f,-0.495565265f, 0.867046237f,-0.498227656f, 0.865513623f,-0.500885367f,
0.863972843f,-0.50353837f, 0.862423956f,-0.506186664f, 0.860866964f,-0.50883013f, 0.859301805f,-0.511468828f,
0.857728601f,-0.514102757f, 0.856147349f,-0.516731799f, 0.854557991f,-0.519356012f, 0.852960587f,-0.521975279f,
0.851355195f,-0.524589658f, 0.849741757f,-0.527199149f, 0.848120332f,-0.529803634f, 0.84649092f,-0.532403111f,
0.84485358f,-0.534997642f, 0.843208253f,-0.537587047f, 0.841554999f,-0.540171444f, 0.839893818f,-0.542750776f,
0.838224709f,-0.545324981f, 0.836547732f,-0.547894061f, 0.834862888f,-0.550457954f, 0.833170176f,-0.553016722f,
0.831469595f,-0.555570245f, 0.829761207f,-0.558118522f, 0.82804507f,-0.560661554f, 0.826321065f,-0.563199341f,
0.824589312f,-0.565731823f, 0.82284981f,-0.568258941f, 0.8211025f,-0.570780754f, 0.819347501f,-0.573297143f,
0.817584813f,-0.575808167f, 0.815814435f,-0.578313768f, 0.81403631f,-0.580813944f, 0.812250614f,-0.583308637f,
0.81045717f,-0.585797846f, 0.808656156f,-0.588281572f, 0.806847572f,-0.590759695f, 0.805031359f,-0.593232274f,
0.803207517f,-0.59569931f, 0.801376164f,-0.598160684f, 0.799537241f,-0.600616455f, 0.797690868f,-0.603066623f,
0.795836926f,-0.605511069f, 0.793975472f,-0.607949793f, 0.792106569f,-0.610382795f, 0.790230215f,-0.612810075f,
0.78834641f,-0.615231574f, 0.786455214f,-0.61764729f, 0.784556568f,-0.620057225f, 0.78265059f,-0.622461259f,
0.780737221f,-0.624859512f, 0.778816521f,-0.627251804f, 0.77688849f,-0.629638255f, 0.774953127f,-0.632018745f,
0.773010433f,-0.634393275f, 0.771060526f,-0.636761844f, 0.769103348f,-0.639124453f, 0.767138898f,-0.641481042f,
0.765167236f,-0.643831551f, 0.763188422f,-0.64617604f, 0.761202395f,-0.64851439f, 0.759209216f,-0.65084666f,
0.757208824f,-0.653172851f, 0.755201399f,-0.655492842f, 0.753186822f,-0.657806695f, 0.751165152f,-0.660114348f,
0.749136388f,-0.662415802f, 0.747100592f,-0.664710999f, 0.745057762f,-0.666999936f, 0.743007958f,-0.669282615f,
0.740951121f,-0.671558976f, 0.73888731f,-0.673829019f, 0.736816585f,-0.676092684f, 0.734738886f,-0.678350031f,
0.732654274f,-0.680601001f, 0.730562747f,-0.682845533f, 0.728464365f,-0.685083687f, 0.726359129f,-0.687315345f,
0.724247098f,-0.689540565f, 0.722128212f,-0.691759229f, 0.720002532f,-0.693971455f, 0.717870057f,-0.696177125f,
0.715730846f,-0.698376238f, 0.71358484f,-0.700568795f, 0.711432219f,-0.702754736f, 0.709272802f,-0.704934061f,
0.707106769f,-0.707106769f, 0.704934061f,-0.709272802f, 0.702754736f,-0.711432219f, 0.700568795f,-0.71358484f,
0.698376238f,-0.715730846f, 0.696177125f,-0.717870057f, 0.693971455f,-0.720002532f, 0.691759229f,-0.722128212f,
0.689540565f,-0.724247098f, 0.687315345f,-0.726359129f, 0.685083687f,-0.728464365f, 0.682845533f,-0.730562747f,
0.680601001f,-0.732654274f, 0.678350031f,-0.734738886f, 0.676092684f,-0.736816585f, 0.673829019f,-0.73888731f,
0.671558976f,-0.740951121f, 0.669282615f,-0.743007958f, 0.666999936f,-0.745057762f, 0.664710999f,-0.747100592f,
0.662415802f,-0.749136388f, 0.660114348f,-0.751165152f, 0.657806695f,-0.753186822f, 0.655492842f,-0.755201399f,
0.653172851f,-0.757208824f, 0.65084666f,-0.759209216f, 0.64851439f,-0.761202395f, 0.64617604f,-0.763188422f,
0.643831551f,-0.765167236f, 0.641481042f,-0.767138898f, 0.639124453f,-0.769103348f, 0.636761844f,-0.771060526f,
0.634393275f,-0.773010433f, 0.632018745f,-0.774953127f, 0.629638255f,-0.77688849f, 0.627251804f,-0.778816521f,
0.624859512f,-0.780737221f, 0.622461259f,-0.78265059f, 0.620057225f,-0.784556568f, 0.61764729f,-0.786455214f,
0.615231574f,-0.78834641f, 0.612810075f,-0.790230215f, 0.610382795f,-0.792106569f, 0.607949793f,-0.793975472f,
0.605511069f,-0.795836926f, 0.603066623f,-0.797690868f, 0.600616455f,-0.799537241f, 0.598160684f,-0.801376164f,
0.59569931f,-0.803207517f, 0.593232274f,-0.805031359f, 0.590759695f,-0.806847572f, 0.588281572f,-0.808656156f,
0.585797846f,-0.81045717f, 0.583308637f,-0.812250614f, 0.580813944f,-0.81403631f, 0.578313768f,-0.815814435f,
0.575808167f,-0.817584813f, 0.573297143f,-0.819347501f, 0.570780754f,-0.8211025f, 0.568258941f,-0.82284981f,
0.565731823f,-0.824589312f, 0.563199341f,-0.826321065f, 0.560661554f,-0.82804507f, 0.558118522f,-0.829761207f,
0.555570245f,-0.831469595f, 0.553016722f,-0.833170176f, 0.550457954f,-0.834862888f, 0.547894061f,-0.836547732f,
0.545324981f,-0.838224709f, 0.542750776f,-0.839893818f, 0.540171444f,-0.841554999f, 0.537587047f,-0.843208253f,
0.534997642f,-0.84485358f, 0.532403111f,-0.84649092f, 0.529803634f,-0.848120332f, 0.527199149f,-0.849741757f,
0.524589658f,-0.851355195f, 0.521975279f,-0.852960587f, 0.519356012f,-0.854557991f, 0.516731799f,-0.856147349f,
0.514102757f,-0.857728601f, 0.511468828f,-0.859301805f, 0.50883013f,-0.860866964f, 0.506186664f,-0.862423956f,
0.50353837f,-0.863972843f, 0.500885367f,-0.865513623f, 0.498227656f,-0.867046237f, 0.495565265f,-0.868570685f,
0.492898196f,-0.870086968f, 0.490226477f,-0.871595085f, 0.487550169f,-0.873094976f, 0.484869242f,-0.874586642f,
0.482183784f,-0.876070082f, 0.479493767f,-0.877545297f, 0.47679922f,-0.879012227f, 0.474100202f,-0.880470872f,
0.471396744f,-0.881921291f, 0.468688816f,-0.883363366f, 0.465976506f,-0.884797096f, 0.463259786f,-0.886222541f,
0.460538715f,-0.887639642f, 0.457813293f,-0.889048338f, 0.455083579f,-0.890448749f, 0.452349573f,-0.891840696f,
0.449611336f,-0.893224299f, 0.446868837f,-0.894599497f, 0.444122136f,-0.895966232f, 0.441371262f,-0.897324562f,
0.438616246f,-0.898674488f, 0.435857087f,-0.900015891f, 0.433093816f,-0.901348829f, 0.430326492f,-0.902673304f,
0.427555084f,-0.903989315f, 0.424779683f,-0.905296743f, 0.422000259f,-0.906595707f, 0.419216901f,-0.907886088f,
0.416429549f,-0.909168005f, 0.413638324f,-0.910441279f, 0.410843164f,-0.91170603f, 0.408044159f,-0.912962198f,
0.405241311f,-0.914209783f, 0.402434647f,-0.915448725f, 0.399624199f,-0.916679084f, 0.396809995f,-0.917900801f,
0.393992037f,-0.919113874f, 0.391170382f,-0.920318305f, 0.388345033f,-0.921514034f, 0.385516047f,-0.92270112f,
0.382683426f,-0.923879504f, 0.379847199f,-0.925049245f, 0.377007425f,-0.926210225f, 0.374164075f,-0.927362502f,
0.371317208f,-0.928506076f, 0.368466824f,-0.929640889f, 0.365612984f,-0.93076694f, 0.362755716f,-0.931884289f,
0.359895051f,-0.932992816f, 0.357030958f,-0.934092522f, 0.354163527f,-0.935183525f, 0.351292759f,-0.936265647f,
0.348418683f,-0.937339008f, 0.345541328f,-0.938403547f, 0.342660725f,-0.939459205f, 0.339776874f,-0.940506041f,
0.336889863f,-0.941544056f, 0.333999664f,-0.94257319f, 0.331106305f,-0.943593442f, 0.328209847f,-0.944604814f,
0.32531029f,-0.945607305f, 0.322407693f,-0.946600914f, 0.319502026f,-0.947585583f, 0.316593379f,-0.94856137f,
0.313681751f,-0.949528158f, 0.310767144f,-0.950486064f, 0.307849646f,-0.95143503f, 0.304929227f,-0.952374995f,
0.302005947f,-0.953306019f, 0.299079835f,-0.954228103f, 0.296150893f,-0.955141187f, 0.293219149f,-0.95604527f,
0.290284663f,-0.956940353f, 0.287347466f,-0.957826436f, 0.284407526f,-0.958703458f, 0.281464934f,-0.95957154f,
0.27851969f,-0.960430503f, 0.275571823f,-0.961280465f, 0.272621363f,-0.962121427f, 0.269668311f,-0.962953269f,
0.266712755f,-0.963776052f, 0.263754666f,-0.964589775f, 0.260794103f,-0.965394437f, 0.257831097f,-0.966189981f,
0.254865646f,-0.966976464f, 0.251897812f,-0.967753828f, 0.248927608f,-0.968522072f, 0.24595505f,-0.969281256f,
0.242980182f,-0.970031261f, 0.24000302f,-0.970772147f, 0.237023607f,-0.971503913f, 0.234041959f,-0.972226501f,
0.231058106f,-0.972939968f, 0.228072077f,-0.973644257f, 0.225083917f,-0.974339366f, 0.222093627f,-0.975025356f,
0.219101235f,-0.975702107f, 0.216106802f,-0.976369739f, 0.213110313f,-0.977028131f, 0.210111842f,-0.977677345f,
0.207111374f,-0.97831738f, 0.204108968f,-0.978948176f, 0.201104641f,-0.979569793f, 0.198098406f,-0.980182111f,
0.195090324f,-0.980785251f, 0.192080393f,-0.981379211f, 0.18906866f,-0.981963873f, 0.186055154f,-0.982539296f,
0.183039889f,-0.983105481f, 0.180022895f,-0.983662426f, 0.177004218f,-0.984210074f, 0.173983872f,-0.984748483f,
0.170961887f,-0.985277653f, 0.167938292f,-0.985797524f, 0.164913118f,-0.986308098f, 0.161886394f,-0.986809373f,
0.15885815f,-0.987301409f, 0.155828401f,-0.987784147f, 0.152797192f,-0.988257587f, 0.149764538f,-0.988721669f,
0.146730468f,-0.989176512f, 0.143695027f,-0.989621997f, 0.140658244f,-0.990058184f, 0.137620121f,-0.990485072f,
0.134580702f,-0.990902662f, 0.13154003f,-0.991310835f, 0.128498107f,-0.991709769f, 0.125454977f,-0.992099285f,
0.122410677f,-0.992479563f, 0.119365215f,-0.992850423f, 0.116318628f,-0.993211925f, 0.113270953f,-0.993564129f,
0.110222206f,-0.993906975f, 0.107172422f,-0.994240463f, 0.104121633f,-0.994564593f, 0.10106986f,-0.994879305f,
0.0980171412f,-0.99518472f, 0.0949634984f,-0.995480776f, 0.0919089541f,-0.995767415f, 0.0888535529f,-0.996044695f,
0.0857973099f,-0.996312618f, 0.0827402622f,-0.996571124f, 0.0796824396f,-0.996820271f, 0.0766238645f,-0.997060061f,
0.0735645667f,-0.997290432f, 0.070504576f,-0.997511446f, 0.0674439222f,-0.997723043f, 0.0643826276f,-0.997925282f,
0.061320737f,-0.998118103f, 0.0582582653f,-0.998301566f, 0.0551952459f,-0.998475552f, 0.052131705f,-0.998640239f,
0.0490676761f,-0.99879545f, 0.0460031815f,-0.998941302f, 0.0429382585f,-0.999077737f, 0.0398729257f,-0.999204755f,
0.0368072242f,-0.999322355f, 0.0337411724f,-0.999430597f, 0.030674804f,-0.999529421f, 0.027608145f,-0.999618828f,
0.024541229f,-0.999698818f, 0.0214740802f,-0.99976939f, 0.0184067301f,-0.999830604f, 0.015339206f,-0.99988234f,
0.0122715384f,-0.999924719f, 0.00920375437f,-0.999957621f, 0.00613588467f,-0.999981165f, 0.00306795677f,-0.999995291f,
0.0f,-1.0f, -0.00306795677f,-0.999995291f, -0.00613588467f,-0.999981165f, -0.00920375437f,-0.999957621f,
-0.0122715384f,-0.999924719f, -0.015339206f,-0.99988234f, -0.0184067301f,-0.999830604f, -0.0214740802f,-0.99976939f,
-0.024541229f,-0.999698818f, -0.027608145f,-0.999618828f, -0.030674804f,-0.999529421f, -0.0337411724f,-0.999430597f,
-0.0368072242f,-0.999322355f, -0.0398729257f,-0.999204755f, -0.0429382585f,-0.999077737f, -0.0460031815f,-0.998941302f,
-0.0490676761f,-0.99879545f, -0.052131705f,-0.998640239f, -0.0551952459f,-0.998475552f, -0.0582582653f,-0.998301566f,
-0.061320737f,-0.998118103f, -0.0643826276f,-0.997925282f, -0.0674439222f,-0.997723043f, -0.070504576f,-0.997511446f,
-0.0735645667f,-0.997290432f, -0.0766238645f,-0.997060061f, -0.0796824396f,-0.996820271f, -0.0827402622f,-0.996571124f,
-0.0857973099f,-0.996312618f, -0.0888535529f,-0.996044695f, -0.0919089541f,-0.995767415f, -0.0949634984f,-0.995480776f,
-0.0980171412f,-0.99518472f, -0.10106986f,-0.994879305f, -0.104121633f,-0.994564593f, -0.107172422f,-0.994240463f,
-0.110222206f,-0.993906975f, -0.113270953f,-0.993564129f, -0.116318628f,-0.993211925f, -0.119365215f,-0.992850423f,
-0.122410677f,-0.992479563f, -0.125454977f,-0.992099285f, -0.128498107f,-0.991709769f, -0.13154003f,-0.991310835f,
-0.134580702f,-0.990902662f, -0.137620121f,-0.990485072f, -0.140658244f,-0.990058184f, -0.143695027f,-0.989621997f,
-0.146730468f,-0.989176512f, -0.149764538f,-0.988721669f, -0.152797192f,-0.988257587f, -0.155828401f,-0.987784147f,
-0.15885815f,-0.987301409f, -0.161886394f,-0.986809373f, -0.164913118f,-0.986308098f, -0.167938292f,-0.985797524f,
-0.170961887f,-0.985277653f, -0.173983872f,-0.984748483f, -0.177004218f,-0.984210074f, -0.180022895f,-0.983662426f,
-0.183039889f,-0.983105481f, -0.186055154f,-0.982539296f, -0.18906866f,-0.981963873f, -0.192080393f,-0.981379211f,
-0.195090324f,-0.980785251f, -0.198098406f,-0.980182111f, -0.201104641f,-0.979569793f, -0.204108968f,-0.978948176f,
-0.207111374f,-0.97831738f, -0.210111842f,-0.977677345f, -0.213110313f,-0.977028131f, -0.216106802f,-0.976369739f,
-0.219101235f,-0.975702107f, -0.222093627f,-0.975025356f, -0.225083917f,-0.974339366f, -0.228072077f,-0.973644257f,
-0.231058106f,-0.972939968f, -0.234041959f,-0.972226501f, -0.237023607f,-0.971503913f, -0.24000302f,-0.970772147f,
-0.242980182f,-0.970031261f, -0.24595505f,-0.969281256f, -0.248927608f,-0.968522072f, -0.251897812f,-0.967753828f,
-0.254865646f,-0.966976464f, -0.257831097f,-0.966189981f, -0.260794103f,-0.965394437f, -0.263754666f,-0.964589775f,
-0.266712755f,-0.963776052f, -0.269668311f,-0.962953269f, -0.272621363f,-0.962121427f, -0.275571823f,-0.961280465f,
-0.27851969f,-0.960430503f, -0.281464934f,-0.95957154f, -0.284407526f,-0.958703458f, -0.287347466f,-0.957826436f,
-0.290284663f,-0.956940353f, -0.293219149f,-0.95604527f, -0.296150893f,-0.955141187f, -0.299079835f,-0.954228103f,
-0.302005947f,-0.953306019f, -0.304929227f,-0.952374995f, -0.307849646f,-0.95143503f, -0.310767144f,-0.950486064f,
-0.313681751f,-0.949528158f, -0.316593379f,-0.94856137f, -0.319502026f,-0.947585583f, -0.322407693f,-0.946600914f,
-0.32531029f,-0.945607305f, -0.328209847f,-0.944604814f, -0.331106305f,-0.943593442f, -0.333999664f,-0.94257319f,
-0.336889863f,-0.941544056f, -0.339776874f,-0.940506041f, -0.342660725f,-0.939459205f, -0.345541328f,-0.938403547f,
-0.348418683f,-0.937339008f, -0.351292759f,-0.936265647f, -0.354163527f,-0.935183525f, -0.357030958f,-0.934092522f,
-0.359895051f,-0.932992816f, -0.362755716f,-0.931884289f, -0.365612984f,-0.93076694f, -0.368466824f,-0.929640889f,
-0.371317208f,-0.928506076f, -0.374164075f,-0.927362502f, -0.377007425f,-0.926210225f, -0.379847199f,-0.925049245f,
-0.382683426f,-0.923879504f, -0.385516047f,-0.92270112f, -0.388345033f,-0.921514034f, -0.391170382f,-0.920318305f,
-0.393992037f,-0.919113874f, -0.396809995f,-0.917900801f, -0.399624199f,-0.916679084f, -0.402434647f,-0.915448725f,
-0.405241311f,-0.914209783f, -0.408044159f,-0.912962198f, -0.410843164f,-0.91170603f, -0.413638324f,-0.910441279f,
-0.416429549f,-0.909168005f, -0.419216901f,-0.907886088f, -0.422000259f,-0.906595707f, -0.424779683f,-0.905296743f,
-0.427555084f,-0.903989315f, -0.430326492f,-0.902673304f, -0.433093816f,-0.901348829f, -0.435857087f,-0.900015891f,
-0.438616246f,-0.898674488f, -0.441371262f,-0.897324562f, -0.444122136f,-0.895966232f, -0.446868837f,-0.894599497f,
-0.449611336f,-0.893224299f, -0.452349573f,-0.891840696f, -0.455083579f,-0.890448749f, -0.457813293f,-0.889048338f,
-0.460538715f,-0.887639642f, -0.463259786f,-0.886222541f, -0.465976506f,-0.884797096f, -0.468688816f,-0.883363366f,
-0.471396744f,-0.881921291f, -0.474100202f,-0.880470872f, -0.47679922f,-0.879012227f, -0.479493767f,-0.877545297f,
-0.482183784f,-0.876070082f, -0.484869242f,-0.874586642f, -0.487550169f,-0.873094976f, -0.490226477f,-0.871595085f,
-0.492898196f,-0.870086968f, -0.495565265f,-0.868570685f, -0.498227656f,-0.867046237f, -0.500885367f,-0.865513623f,
-0.50353837f,-0.863972843f, -0.506186664f,-0.862423956f, -0.50883013f,-0.860866964f, -0.511468828f,-0.859301805f,
-0.514102757f,-0.857728601f, -0.516731799f,-0.856147349f, -0.519356012f,-0.854557991f, -0.521975279f,-0.852960587f,
-0.524589658f,-0.851355195f, -0.527199149f,-0.849741757f, -0.529803634f,-0.848120332f, -0.532403111f,-0.84649092f,
-0.534997642f,-0.84485358f, -0.537587047f,-0.843208253f, -0.540171444f,-0.841554999f, -0.542750776f,-0.839893818f,
-0.545324981f,-0.838224709f, -0.547894061f,-0.836547732f, -0.550457954f,-0.834862888f, -0.553016722f,-0.833170176f,
-0.555570245f,-0.831469595f, -0.558118522f,-0.829761207f, -0.560661554f,-0.82804507f, -0.563199341f,-0.826321065f,
-0.565731823f,-0.824589312f, -0.568258941f,-0.82284981f, -0.570780754f,-0.8211025f, -0.573297143f,-0.819347501f,
-0.575808167f,-0.817584813f, -0.578313768f,-0.815814435f, -0.580813944f,-0.81403631f, -0.583308637f,-0.812250614f,
-0.585797846f,-0.81045717f, -0.588281572f,-0.808656156f, -0.590759695f,-0.806847572f, -0.593232274f,-0.805031359f,
-0.59569931f,-0.803207517f, -0.598160684f,-0.801376164f, -0.600616455f,-0.799537241f, -0.603066623f,-0.797690868f,
-0.605511069f,-0.795836926f, -0.607949793f,-0.793975472f, -0.610382795f,-0.792106569f, -0.612810075f,-0.790230215f,
-0.615231574f,-0.78834641f, -0.61764729f,-0.786455214f, -0.620057225f,-0.784556568f, -0.622461259f,-0.78265059f,
-0.624859512f,-0.780737221f, -0.627251804f,-0.778816521f, -0.629638255f,-0.77688849f, -0.632018745f,-0.774953127f,
-0.634393275f,-0.773010433f, -0.636761844f,-0.771060526f, -0.639124453f,-0.769103348f, -0.641481042f,-0.767138898f,
-0.643831551f,-0.765167236f, -0.64617604f,-0.763188422f, -0.64851439f,-0.761202395f, -0.65084666f,-0.759209216f,
-0.653172851f,-0.757208824f, -0.655492842f,-0.755201399f, -0.657806695f,-0.753186822f, -0.660114348f,-0.751165152f,
-0.662415802f,-0.749136388f, -0.664710999f,-0.747100592f, -0.666999936f,-0.745057762f, -0.669282615f,-0.743007958f,
-0.671558976f,-0.740951121f, -0.673829019f,-0.73888731f, -0.676092684f,-0.736816585f, -0.678350031f,-0.734738886f,
-0.680601001f,-0.732654274f, -0.682845533f,-0.730562747f, -0.685083687f,-0.728464365f, -0.687315345f,-0.726359129f,
-0.689540565f,-0.724247098f, -0.691759229f,-0.722128212f, -0.693971455f,-0.720002532f, -0.696177125f,-0.717870057f,
-0.698376238f,-0.715730846f, -0.700568795f,-0.71358484f, -0.702754736f,-0.711432219f, -0.704934061f,-0.709272802f,
-0.707106769f,-0.707106769f, -0.709272802f,-0.704934061f, -0.711432219f,-0.702754736f, -0.71358484f,-0.700568795f,
-0.715730846f,-0.698376238f, -0.717870057f,-0.696177125f, -0.720002532f,-0.693971455f, -0.722128212f,-0.691759229f,
-0.724247098f,-0.689540565f, -0.726359129f,-0.687315345f, -0.728464365f,-0.685083687f, -0.730562747f,-0.682845533f,
-0.732654274f,-0.680601001f, -0.734738886f,-0.678350031f, -0.736816585f,-0.676092684f, -0.73888731f,-0.673829019f,
-0.740951121f,-0.671558976f, -0.743007958f,-0.669282615f, -0.745057762f,-0.666999936f, -0.747100592f,-0.664710999f,
-0.749136388f,-0.662415802f, -0.751165152f,-0.660114348f, -0.753186822f,-0.657806695f, -0.755201399f,-0.655492842f,
-0.757208824f,-0.653172851f, -0.759209216f,-0.65084666f, -0.761202395f,-0.64851439f, -0.763188422f,-0.64617604f,
-0.765167236f,-0.643831551f, -0.767138898f,-0.641481042f, -0.769103348f,-0.639124453f, -0.771060526f,-0.636761844f,
-0.773010433f,-0.634393275f, -0.774953127f,-0.632018745f, -0.77688849f,-0.629638255f, -0.778816521f,-0.627251804f,
-0.780737221f,-0.624859512f, -0.78265059f,-0.622461259f, -0.784556568f,-0.620057225f, -0.786455214f,-0.61764729f,
-0.78834641f,-0.615231574f, -0.790230215f,-0.612810075f, -0.792106569f,-0.610382795f, -0.793975472f,-0.607949793f,
-0.795836926f,-0.605511069f, -0.797690868f,-0.603066623f, -0.799537241f,-0.600616455f, -0.801376164f,-0.598160684f,
-0.803207517f,-0.59569931f, -0.805031359f,-0.593232274f, -0.806847572f,-0.590759695f, -0.808656156f,-0.588281572f,
-0.81045717f,-0.585797846f, -0.812250614f,-0.583308637f, -0.81403631f,-0.580813944f, -0.815814435f,-0.578313768f,
-0.817584813f,-0.575808167f, -0.819347501f,-0.573297143f, -0.8211025f,-0.570780754f, -0.82284981f,-0.568258941f,
-0.824589312f,-0.565731823f, -0.826321065f,-0.563199341f, -0.82804507f,-0.560661554f, -0.829761207f,-0.558118522f,
-0.831469595f,-0.555570245f, -0.833170176f,-0.553016722f, -0.834862888f,-0.550457954f, -0.836547732f,-0.547894061f,
-0.838224709f,-0.545324981f, -0.839893818f,-0.542750776f, -0.841554999f,-0.540171444f, -0.843208253f,-0.537587047f,
-0.84485358f,-0.534997642f, -0.84649092f,-0.532403111f, -0.848120332f,-0.529803634f, -0.849741757f,-0.527199149f,
-0.851355195f,-0.524589658f, -0.852960587f,-0.521975279f, -0.854557991f,-0.519356012f, -0.856147349f,-0.516731799f,
-0.857728601f,-0.514102757f, -0.859301805f,-0.511468828f, -0.860866964f,-0.50883013f, -0.862423956f,-0.506186664f,
-0.863972843f,-0.50353837f, -0.865513623f,-0.500885367f, -0.867046237f,-0.498227656f, -0.868570685f,-0.495565265f,
-0.870086968f,-0.492898196f, -0.871595085f,-0.490226477f, -0.873094976f,-0.487550169f, -0.874586642f,-0.484869242f,
-0.876070082f,-0.482183784f, -0.877545297f,-0.479493767f, -0.879012227f,-0.47679922f, -0.880470872f,-0.474100202f,
-0.881921291f,-0.471396744f, -0.883363366f,-0.468688816f, -0.884797096f,-0.465976506f, -0.886222541f,-0.463259786f,
-0.887639642f,-0.460538715f, -0.889048338f,-0.457813293f, -0.890448749f,-0.455083579f, -0.891840696f,-0.452349573f,
-0.893224299f,-0.449611336f, -0.894599497f,-0.446868837f, -0.895966232f,-0.444122136f, -0.897324562f,-0.441371262f,
-0.898674488f,-0.438616246f, -0.900015891f,-0.435857087f, -0.901348829f,-0.433093816f, -0.902673304f,-0.430326492f,
-0.903989315f,-0.427555084f, -0.905296743f,-0.424779683f, -0.906595707f,-0.422000259f, -0.907886088f,-0.419216901f,
-0.909168005f,-0.416429549f, -0.910441279f,-0.413638324f, -0.91170603f,-0.410843164f, -0.912962198f,-0.408044159f,
-0.914209783f,-0.405241311f, -0.915448725f,-0.402434647f, -0.916679084f,-0.399624199f, -0.917900801f,-0.396809995f,
-0.919113874f,-0.393992037f, -0.920318305f,-0.391170382f, -0.921514034f,-0.388345033f, -0.92270112f,-0.385516047f,
-0.923879504f,-0.382683426f, -0.925049245f,-0.379847199f, -0.926210225f,-0.377007425f, -0.927362502f,-0.374164075f,
-0.928506076f,-0.371317208f, -0.929640889f,-0.368466824f, -0.93076694f,-0.365612984f, -0.931884289f,-0.362755716f,
-0.932992816f,-0.359895051f, -0.934092522f,-0.357030958f, -0.935183525f,-0.354163527f, -0.936265647f,-0.351292759f,
-0.937339008f,-0.348418683f, -0.938403547f,-0.345541328f, -0.939459205f,-0.342660725f, -0.940506041f,-0.339776874f,
-0.941544056f,-0.336889863f, -0.94257319f,-0.333999664f, -0.943593442f,-0.331106305f, -0.944604814f,-0.328209847f,
-0.945607305f,-0.32531029f, -0.946600914f,-0.322407693f, -0.947585583f,-0.319502026f, -0.94856137f,-0.316593379f,
-0.949528158f,-0.313681751f, -0.950486064f,-0.310767144f, -0.95143503f,-0.307849646f, -0.952374995f,-0.304929227f,
-0.953306019f,-0.302005947f, -0.954228103f,-0.299079835f, -0.955141187f,-0.296150893f, -0.95604527f,-0.293219149f,
-0.956940353f,-0.290284663f, -0.957826436f,-0.287347466f, -0.958703458f,-0.284407526f, -0.95957154f,-0.281464934f,
-0.960430503f,-0.27851969f, -0.961280465f,-0.275571823f, -0.962121427f,-0.272621363f, -0.962953269f,-0.269668311f,
-0.963776052f,-0.266712755f, -0.964589775f,-0.263754666f, -0.965394437f,-0.260794103f, -0.966189981f,-0.257831097f,
-0.966976464f,-0.254865646f, -0.967753828f,-0.251897812f, -0.968522072f,-0.248927608f, -0.969281256f,-0.24595505f,
-0.970031261f,-0.242980182f, -0.970772147f,-0.24000302f, -0.971503913f,-0.237023607f, -0.972226501f,-0.234041959f,
-0.972939968f,-0.231058106f, -0.973644257f,-0.228072077f, -0.974339366f,-0.225083917f, -0.975025356f,-0.222093627f,
-0.975702107f,-0.219101235f, -0.976369739f,-0.216106802f, -0.977028131f,-0.213110313f, -0.977677345f,-0.210111842f,
-0.97831738f,-0.207111374f, -0.978948176f,-0.204108968f, -0.979569793f,-0.201104641f, -0.980182111f,-0.198098406f,
-0.980785251f,-0.195090324f, -0.981379211f,-0.192080393f, -0.981963873f,-0.18906866f, -0.982539296f,-0.186055154f,
-0.983105481f,-0.183039889f, -0.983662426f,-0.180022895f, -0.984210074f,-0.177004218f, -0.984748483f,-0.173983872f,
-0.985277653f,-0.170961887f, -0.985797524f,-0.167938292f, -0.986308098f,-0.164913118f, -0.986809373f,-0.161886394f,
-0.987301409f,-0.15885815f, -0.987784147f,-0.155828401f, -0.988257587f,-0.152797192f, -0.988721669f,-0.149764538f,
-0.989176512f,-0.146730468f, -0.989621997f,-0.143695027f, -0.990058184f,-0.140658244f, -0.990485072f,-0.137620121f,
-0.990902662f,-0.134580702f, -0.991310835f,-0.13154003f, -0.991709769f,-0.128498107f, -0.992099285f,-0.125454977f,
-0.992479563f,-0.122410677f, -0.992850423f,-0.119365215f, -0.993211925f,-0.116318628f, -0.993564129f,-0.113270953f,
-0.993906975f,-0.110222206f, -0.994240463f,-0.107172422f, -0.994564593f,-0.104121633f, -0.994879305f,-0.10106986f,
-0.99518472f,-0.0980171412f, -0.995480776f,-0.0949634984f, -0.995767415f,-0.0919089541f, -0.996044695f,-0.0888535529f,
-0.996312618f,-0.0857973099f, -0.996571124f,-0.0827402622f, -0.996820271f,-0.0796824396f, -0.997060061f,-0.0766238645f,
-0.997290432f,-0.0735645667f, -0.997511446f,-0.070504576f, -0.997723043f,-0.0674439222f, -0.997925282f,-0.0643826276f,
-0.998118103f,-0.061320737f, -0.998301566f,-0.0582582653f, -0.998475552f,-0.0551952459f, -0.998640239f,-0.052131705f,
-0.99879545f,-0.0490676761f, -0.998941302f,-0.0460031815f, -0.999077737f,-0.0429382585f, -0.999204755f,-0.0398729257f,
-0.999322355f,-0.0368072242f, -0.999430597f,-0.0337411724f, -0.999529421f,-0.030674804f, -0.999618828f,-0.027608145f,
-0.999698818f,-0.024541229f, -0.99976939f,-0.0214740802f, -0.999830604f,-0.0184067301f, -0.99988234f,-0.015339206f,
-0.999924719f,-0.0122715384f, -0.999957621f,-0.00920375437f, -0.999981165f,-0.00613588467f, -0.999995291f,-0.00306795677f,
-1.0f,0.0f, -0.999995291f,0.00306795677f, -0.999981165f,0.00613588467f, -0.999957621f,0.00920375437f,
-0.999924719f,0.0122715384f, -0.99988234f,0.015339206f, -0.999830604f,0.0184067301f, -0.99976939f,0.0214740802f,
-0.999698818f,0.024541229f, -0.999618828f,0.027608145f, -0.999529421f,0.030674804f, -0.999430597f,0.0337411724f,
-0.999322355f,0.0368072242f, -0.999204755f,0.0398729257f, -0.999077737f,0.0429382585f, -0.998941302f,0.0460031815f,
-0.99879545f,0.0490676761f, -0.998640239f,0.052131705f, -0.998475552f,0.0551952459f, -0.998301566f,0.0582582653f,
-0.998118103f,0.061320737f, -0.997925282f,0.0643826276f, -0.997723043f,0.0674439222f, -0.997511446f,0.070504576f,
-0.997290432f,0.0735645667f, -0.997060061f,0.0766238645f, -0.996820271f,0.0796824396f, -0.996571124f,0.0827402622f,
-0.996312618f,0.0857973099f, -0.996044695f,0.0888535529f, -0.995767415f,0.0919089541f, -0.995480776f,0.0949634984f,
-0.99518472f,0.0980171412f, -0.994879305f,0.10106986f, -0.994564593f,0.104121633f, -0.994240463f,0.107172422f,
-0.993906975f,0.110222206f, -0.993564129f,0.113270953f, -0.993211925f,0.116318628f, -0.992850423f,0.119365215f,
-0.992479563f,0.122410677f, -0.992099285f,0.125454977f, -0.991709769f,0.128498107f, -0.991310835f,0.13154003f,
-0.990902662f,0.134580702f, -0.990485072f,0.137620121f, -0.990058184f,0.140658244f, -0.989621997f,0.143695027f,
-0.989176512f,0.146730468f, -0.988721669f,0.149764538f, -0.988257587f,0.152797192f, -0.987784147f,0.155828401f,
-0.987301409f,0.15885815f, -0.986809373f,0.161886394f, -0.986308098f,0.164913118f, -0.985797524f,0.167938292f,
-0.985277653f,0.170961887f, -0.984748483f,0.173983872f, -0.984210074f,0.177004218f, -0.983662426f,0.180022895f,
-0.983105481f,0.183039889f, -0.982539296f,0.186055154f, -0.981963873f,0.18906866f, -0.981379211f,0.192080393f,
-0.980785251f,0.195090324f, -0.980182111f,0.198098406f, -0.979569793f,0.201104641f, -0.978948176f,0.204108968f,
-0.97831738f,0.207111374f, -0.977677345f,0.210111842f, -0.977028131f,0.213110313f, -0.976369739f,0.216106802f,
-0.975702107f,0.219101235f, -0.975025356f,0.222093627f, -0.974339366f,0.225083917f, -0.973644257f,0.228072077f,
-0.972939968f,0.231058106f, -0.972226501f,0.234041959f, -0.971503913f,0.237023607f, -0.970772147f,0.24000302f,
-0.970031261f,0.242980182f, -0.969281256f,0.24595505f, -0.968522072f,0.248927608f, -0.967753828f,0.251897812f,
-0.966976464f,0.254865646f, -0.966189981f,0.257831097f, -0.965394437f,0.260794103f, -0.964589775f,0.263754666f,
-0.963776052f,0.266712755f, -0.962953269f,0.269668311f, -0.962121427f,0.272621363f, -0.961280465f,0.275571823f,
-0.960430503f,0.27851969f, -0.95957154f,0.281464934f, -0.958703458f,0.284407526f, -0.957826436f,0.287347466f,
-0.956940353f,0.290284663f, -0.95604527f,0.293219149f, -0.955141187f,0.296150893f, -0.954228103f,0.299079835f,
-0.953306019f,0.302005947f, -0.952374995f,0.304929227f, -0.95143503f,0.307849646f, -0.950486064f,0.310767144f,
-0.949528158f,0.313681751f, -0.94856137f,0.316593379f, -0.947585583f,0.319502026f, -0.946600914f,0.322407693f,
-0.945607305f,0.32531029f, -0.944604814f,0.328209847f, -0.943593442f,0.331106305f, -0.94257319f,0.333999664f,
-0.941544056f,0.336889863f, -0.940506041f,0.339776874f, -0.939459205f,0.342660725f, -0.938403547f,0.345541328f,
-0.937339008f,0.348418683f, -0.936265647f,0.351292759f, -0.935183525f,0.354163527f, -0.934092522f,0.357030958f,
-0.932992816f,0.359895051f, -0.931884289f,0.362755716f, -0.93076694f,0.365612984f, -0.929640889f,0.368466824f,
-0.928506076f,0.371317208f, -0.927362502f,0.374164075f, -0.926210225f,0.377007425f, -0.925049245f,0.379847199f,
-0.923879504f,0.382683426f, -0.92270112f,0.385516047f, -0.921514034f,0.388345033f, -0.920318305f,0.391170382f,
-0.919113874f,0.393992037f, -0.917900801f,0.396809995f, -0.916679084f,0.399624199f, -0.915448725f,0.402434647f,
-0.914209783f,0.405241311f, -0.912962198f,0.408044159f, -0.91170603f,0.410843164f, -0.910441279f,0.413638324f,
-0.909168005f,0.416429549f, -0.907886088f,0.419216901f, -0.906595707f,0.422000259f, -0.905296743f,0.424779683f,
-0.903989315f,0.427555084f, -0.902673304f,0.430326492f, -0.901348829f,0.433093816f, -0.900015891f,0.435857087f,
-0.898674488f,0.438616246f, -0.897324562f,0.441371262f, -0.895966232f,0.444122136f, -0.894599497f,0.446868837f,
-0.893224299f,0.449611336f, -0.891840696f,0.452349573f, -0.890448749f,0.455083579f, -0.889048338f,0.457813293f,
-0.887639642f,0.460538715f, -0.886222541f,0.463259786f, -0.884797096f,0.465976506f, -0.883363366f,0.468688816f,
-0.881921291f,0.471396744f, -0.880470872f,0.474100202f, -0.879012227f,0.47679922f, -0.877545297f,0.479493767f,
-0.876070082f,0.482183784f, -0.874586642f,0.484869242f, -0.873094976f,0.487550169f, -0.871595085f,0.490226477f,
-0.870086968f,0.492898196f, -0.868570685f,0.495565265f, -0.867046237f,0.498227656f, -0.865513623f,0.500885367f,
-0.863972843f,0.50353837f, -0.862423956f,0.506186664f, -0.860866964f,0.50883013f, -0.859301805f,0.511468828f,
-0.857728601f,0.514102757f, -0.856147349f,0.516731799f, -0.854557991f,0.519356012f, -0.852960587f,0.521975279f,
-0.851355195f,0.524589658f, -0.849741757f,0.527199149f, -0.848120332f,0.529803634f, -0.84649092f,0.532403111f,
-0.84485358f,0.534997642f, -0.843208253f,0.537587047f, -0.841554999f,0.540171444f, -0.839893818f,0.542750776f,
-0.838224709f,0.545324981f, -0.836547732f,0.547894061f, -0.834862888f,0.550457954f, -0.833170176f,0.553016722f,
-0.831469595f,0.555570245f, -0.829761207f,0.558118522f, -0.82804507f,0.560661554f, -0.826321065f,0.563199341f,
-0.824589312f,0.565731823f, -0.82284981f,0.568258941f, -0.8211025f,0.570780754f, -0.819347501f,0.573297143f,
-0.817584813f,0.575808167f, -0.815814435f,0.578313768f, -0.81403631f,0.580813944f, -0.812250614f,0.583308637f,
-0.81045717f,0.585797846f, -0.808656156f,0.588281572f, -0.806847572f,0.590759695f, -0.805031359f,0.593232274f,
-0.803207517f,0.59569931f, -0.801376164f,0.598160684f, -0.799537241f,0.600616455f, -0.797690868f,0.603066623f,
-0.795836926f,0.605511069f, -0.793975472f,0.607949793f, -0.792106569f,0.610382795f, -0.790230215f,0.612810075f,
-0.78834641f,0.615231574f, -0.786455214f,0.61764729f, -0.784556568f,0.620057225f, -0.78265059f,0.622461259f,
-0.780737221f,0.624859512f, -0.778816521f,0.627251804f, -0.77688849f,0.629638255f, -0.774953127f,0.632018745f,
-0.773010433f,0.634393275f, -0.771060526f,0.636761844f, -0.769103348f,0.639124453f, -0.767138898f,0.641481042f,
-0.765167236f,0.643831551f, -0.763188422f,0.64617604f, -0.761202395f,0.64851439f, -0.759209216f,0.65084666f,
-0.757208824f,0.653172851f, -0.755201399f,0.655492842f, -0.753186822f,0.657806695f, -0.751165152f,0.660114348f,
-0.749136388f,0.662415802f, -0.747100592f,0.664710999f, -0.745057762f,0.666999936f, -0.743007958f,0.669282615f,
-0.740951121f,0.671558976f, -0.73888731f,0.673829019f, -0.736816585f,0.676092684f, -0.734738886f,0.678350031f,
-0.732654274f,0.680601001f, -0.730562747f,0.682845533f, -0.728464365f,0.685083687f, -0.726359129f,0.687315345f,
-0.724247098f,0.689540565f, -0.722128212f,0.691759229f, -0.720002532f,0.693971455f, -0.717870057f,0.696177125f,
-0.715730846f,0.698376238f, -0.71358484f,0.700568795f, -0.711432219f,0.702754736f, -0.709272802f,0.704934061f,
-0.707106769f,0.707106769f, -0.704934061f,0.709272802f, -0.702754736f,0.711432219f, -0.700568795f,0.71358484f,
-0.698376238f,0.715730846f, -0.696177125f,0.717870057f, -0.693971455f,0.720002532f, -0.691759229f,0.722128212f,
-0.689540565f,0.724247098f, -0.687315345f,0.726359129f, -0.685083687f,0.728464365f, -0.682845533f,0.730562747f,
-0.680601001f,0.732654274f, -0.678350031f,0.734738886f, -0.676092684f,0.736816585f, -0.673829019f,0.73888731f,
-0.671558976f,0.740951121f, -0.669282615f,0.743007958f, -0.666999936f,0.745057762f, -0.664710999f,0.747100592f,
-0.662415802f,0.749136388f, -0.660114348f,0.751165152f, -0.657806695f,0.753186822f, -0.655492842f,0.755201399f,
-0.653172851f,0.757208824f, -0.65084666f,0.759209216f, -0.64851439f,0.761202395f, -0.64617604f,0.763188422f,
-0.643831551f,0.765167236f, -0.641481042f,0.767138898f, -0.639124453f,0.769103348f, -0.636761844f,0.771060526f,
-0.634393275f,0.773010433f, -0.632018745f,0.774953127f, -0.629638255f,0.77688849f, -0.627251804f,0.778816521f,
-0.624859512f,0.780737221f, -0.622461259f,0.78265059f, -0.620057225f,0.784556568f, -0.61764729f,0.786455214f,
-0.615231574f,0.78834641f, -0.612810075f,0.790230215f, -0.610382795f,0.792106569f, -0.607949793f,0.793975472f,
-0.605511069f,0.795836926f, -0.603066623f,0.797690868f, -0.600616455f,0.799537241f, -0.598160684f,0.801376164f,
-0.59569931f,0.803207517f, -0.593232274f,0.805031359f, -0.590759695f,0.806847572f, -0.588281572f,0.808656156f,
-0.585797846f,0.81045717f, -0.583308637f,0.812250614f, -0.580813944f,0.81403631f, -0.578313768f,0.815814435f,
-0.575808167f,0.817584813f, -0.573297143f,0.819347501f, -0.570780754f,0.8211025f, -0.568258941f,0.82284981f,
-0.565731823f,0.824589312f, -0.563199341f,0.826321065f, -0.560661554f,0.82804507f, -0.558118522f,0.829761207f,
-0.555570245f,0.831469595f, -0.553016722f,0.833170176f, -0.550457954f,0.834862888f, -0.547894061f,0.836547732f,
-0.545324981f,0.838224709f, -0.542750776f,0.839893818f, -0.540171444f,0.841554999f, -0.537587047f,0.843208253f,
-0.534997642f,0.84485358f, -0.532403111f,0.84649092f, -0.529803634f,0.848120332f, -0.527199149f,0.849741757f,
-0.524589658f,0.851355195f, -0.521975279f,0.852960587f, -0.519356012f,0.854557991f, -0.516731799f,0.856147349f,
-0.514102757f,0.857728601f, -0.511468828f,0.859301805f, -0.50883013f,0.860866964f, -0.506186664f,0.862423956f,
-0.50353837f,0.863972843f, -0.500885367f,0.865513623f, -0.498227656f,0.867046237f, -0.495565265f,0.868570685f,
-0.492898196f,0.870086968f, -0.490226477f,0.871595085f, -0.487550169f,0.873094976f, -0.484869242f,0.874586642f,
-0.482183784f,0.876070082f, -0.479493767f,0.877545297f, -0.47679922f,0.879012227f, -0.474100202f,0.880470872f,
-0.471396744f,0.881921291f, -0.468688816f,0.883363366f, -0.465976506f,0.884797096f, -0.463259786f,0.886222541f,
-0.460538715f,0.887639642f, -0.457813293f,0.889048338f, -0.455083579f,0.890448749f, -0.452349573f,0.891840696f,
-0.449611336f,0.893224299f, -0.446868837f,0.894599497f, -0.444122136f,0.895966232f, -0.441371262f,0.897324562f,
-0.438616246f,0.898674488f, -0.435857087f,0.900015891f, -0.433093816f,0.901348829f, -0.430326492f,0.902673304f,
-0.427555084f,0.903989315f, -0.424779683f,0.905296743f, -0.422000259f,0.906595707f, -0.419216901f,0.907886088f,
-0.416429549f,0.909168005f, -0.413638324f,0.910441279f, -0.410843164f,0.91170603f, -0.408044159f,0.912962198f,
-0.405241311f,0.914209783f, -0.402434647f,0.915448725f, -0.399624199f,0.916679084f, -0.396809995f,0.917900801f,
-0.393992037f,0.919113874f, -0.391170382f,0.920318305f, -0.388345033f,0.921514034f, -0.385516047f,0.92270112f,
-0.382683426f,0.923879504f, -0.379847199f,0.925049245f, -0.377007425f,0.926210225f, -0.374164075f,0.927362502f,
-0.371317208f,0.928506076f, -0.368466824f,0.929640889f, -0.365612984f,0.93076694f, -0.362755716f,0.931884289f,
-0.359895051f,0.932992816f, -0.357030958f,0.934092522f, -0.354163527f,0.935183525f, -0.351292759f,0.936265647f,
-0.348418683f,0.937339008f, -0.345541328f,0.938403547f, -0.342660725f,0.939459205f, -0.339776874f,0.940506041f,
-0.336889863f,0.941544056f, -0.333999664f,0.94257319f, -0.331106305f,0.943593442f, -0.328209847f,0.944604814f,
-0.32531029f,0.945607305f, -0.322407693f,0.946600914f, -0.319502026f,0.947585583f, -0.316593379f,0.94856137f,
-0.313681751f,0.949528158f, -0.310767144f,0.950486064f, -0.307849646f,0.95143503f, -0.304929227f,0.952374995f,
-0.302005947f,0.953306019f, -0.299079835f,0.954228103f, -0.296150893f,0.955141187f, -0.293219149f,0.95604527f,
-0.290284663f,0.956940353f, -0.287347466f,0.957826436f, -0.284407526f,0.958703458f, -0.281464934f,0.95957154f,
-0.27851969f,0.960430503f, -0.275571823f,0.961280465f, -0.272621363f,0.962121427f, -0.269668311f,0.962953269f,
-0.266712755f,0.963776052f, -0.263754666f,0.964589775f, -0.260794103f,0.965394437f, -0.257831097f,0.966189981f,
-0.254865646f,0.966976464f, -0.251897812f,0.967753828f, -0.248927608f,0.968522072f, -0.24595505f,0.969281256f,
-0.242980182f,0.970031261f, -0.24000302f,0.970772147f, -0.237023607f,0.971503913f, -0.234041959f,0.972226501f,
-0.231058106f,0.972939968f, -0.228072077f,0.973644257f, -0.225083917f,0.974339366f, -0.222093627f,0.975025356f,
-0.219101235f,0.975702107f, -0.216106802f,0.976369739f, -0.213110313f,0.977028131f, -0.210111842f,0.977677345f,
-0.207111374f,0.97831738f, -0.204108968f,0.978948176f, -0.201104641f,0.979569793f, -0.198098406f,0.980182111f,
-0.195090324f,0.980785251f, -0.192080393f,0.981379211f, -0.18906866f,0.981963873f, -0.186055154f,0.982539296f,
-0.183039889f,0.983105481f, -0.180022895f,0.983662426f, -0.177004218f,0.984210074f, -0.173983872f,0.984748483f,
-0.170961887f,0.985277653f, -0.167938292f,0.985797524f, -0.164913118f,0.986308098f, -0.161886394f,0.986809373f,
-0.15885815f,0.987301409f, -0.155828401f,0.987784147f, -0.152797192f,0.988257587f, -0.149764538f,0.988721669f,
-0.146730468f,0.989176512f, -0.143695027f,0.989621997f, -0.140658244f,0.990058184f, -0.137620121f,0.990485072f,
-0.134580702f,0.990902662f, -0.13154003f,0.991310835f, -0.128498107f,0.991709769f, -0.125454977f,0.992099285f,
-0.122410677f,0.992479563f, -0.119365215f,0.992850423f, -0.116318628f,0.993211925f, -0.113270953f,0.993564129f,
-0.110222206f,0.993906975f, -0.107172422f,0.994240463f, -0.104121633f,0.994564593f, -0.10106986f,0.994879305f,
-0.0980171412f,0.99518472f, -0.0949634984f,0.995480776f, -0.0919089541f,0.995767415f, -0.0888535529f,0.996044695f,
-0.0857973099f,0.996312618f, -0.0827402622f,0.996571124f, -0.0796824396f,0.996820271f, -0.0766238645f,0.997060061f,
-0.0735645667f,0.997290432f, -0.070504576f,0.997511446f, -0.0674439222f,0.997723043f, -0.0643826276f,0.997925282f,
-0.061320737f,0.998118103f, -0.0582582653f,0.998301566f, -0.0551952459f,0.998475552f, -0.052131705f,0.998640239f,
-0.0490676761f,0.99879545f, -0.0460031815f,0.998941302f, -0.0429382585f,0.999077737f, -0.0398729257f,0.999204755f,
-0.0368072242f,0.999322355f, -0.0337411724f,0.999430597f, -0.030674804f,0.999529421f, -0.027608145f,0.999618828f,
-0.024541229f,0.999698818f, -0.0214740802f,0.99976939f, -0.0184067301f,0.999830604f, -0.015339206f,0.99988234f,
-0.0122715384f,0.999924719f, -0.00920375437f,0.999957621f, -0.00613588467f,0.999981165f, -0.00306795677f,0.999995291f,
0.0f,1.0f, 0.00306795677f,0.999995291f, 0.00613588467f,0.999981165f, 0.00920375437f,0.999957621f,
0.0122715384f,0.999924719f, 0.015339206f,0.99988234f, 0.0184067301f,0.999830604f, 0.0214740802f,0.99976939f,
0.024541229f,0.999698818f, 0.027608145f,0.999618828f, 0.030674804f,0.999529421f, 0.0337411724f,0.999430597f,
0.0368072242f,0.999322355f, 0.0398729257f,0.999204755f, 0.0429382585f,0.999077737f, 0.0460031815f,0.998941302f,
0.0490676761f,0.99879545f, 0.052131705f,0.998640239f, 0.0551952459f,0.998475552f, 0.0582582653f,0.998301566f,
0.061320737f,0.998118103f, 0.0643826276f,0.997925282f, 0.0674439222f,0.997723043f, 0.070504576f,0.997511446f,
0.0735645667f,0.997290432f, 0.0766238645f,0.997060061f, 0.0796824396f,0.996820271f, 0.0827402622f,0.996571124f,
0.0857973099f,0.996312618f, 0.0888535529f,0.996044695f, 0.0919089541f,0.995767415f, 0.0949634984f,0.995480776f,
0.0980171412f,0.99518472f, 0.10106986f,0.994879305f, 0.104121633f,0.994564593f, 0.107172422f,0.994240463f,
0.110222206f,0.993906975f, 0.113270953f,0.993564129f, 0.116318628f,0.993211925f, 0.119365215f,0.992850423f,
0.122410677f,0.992479563f, 0.125454977f,0.992099285f, 0.128498107f,0.991709769f, 0.13154003f,0.991310835f,
0.134580702f,0.990902662f, 0.137620121f,0.990485072f, 0.140658244f,0.990058184f, 0.143695027f,0.989621997f,
0.146730468f,0.989176512f, 0.149764538f,0.988721669f, 0.152797192f,0.988257587f, 0.155828401f,0.987784147f,
0.15885815f,0.987301409f, 0.161886394f,0.986809373f, 0.164913118f,0.986308098f, 0.167938292f,0.985797524f,
0.170961887f,0.985277653f, 0.173983872f,0.984748483f, 0.177004218f,0.984210074f, 0.180022895f,0.983662426f,
0.183039889f,0.983105481f, 0.186055154f,0.982539296f, 0.18906866f,0.981963873f, 0.192080393f,0.981379211f,
0.195090324f,0.980785251f, 0.198098406f,0.980182111f, 0.201104641f,0.979569793f, 0.204108968f,0.978948176f,
0.207111374f,0.97831738f, 0.210111842f,0.977677345f, 0.213110313f,0.977028131f, 0.216106802f,0.976369739f,
0.219101235f,0.975702107f, 0.222093627f,0.975025356f, 0.225083917f,0.974339366f, 0.228072077f,0.973644257f,
0.231058106f,0.972939968f, 0.234041959f,0.972226501f, 0.237023607f,0.971503913f, 0.24000302f,0.970772147f,
0.242980182f,0.970031261f, 0.24595505f,0.969281256f, 0.248927608f,0.968522072f, 0.251897812f,0.967753828f,
0.254865646f,0.966976464f, 0.257831097f,0.966189981f, 0.260794103f,0.965394437f, 0.263754666f,0.964589775f,
0.266712755f,0.963776052f, 0.269668311f,0.962953269f, 0.272621363f,0.962121427f, 0.275571823f,0.961280465f,
0.27851969f,0.960430503f, 0.281464934f,0.95957154f, 0.284407526f,0.958703458f, 0.287347466f,0.957826436f,
0.290284663f,0.956940353f, 0.293219149f,0.95604527f, 0.296150893f,0.955141187f, 0.299079835f,0.954228103f,
0.302005947f,0.953306019f, 0.304929227f,0.952374995f, 0.307849646f,0.95143503f, 0.310767144f,0.950486064f,
0.313681751f,0.949528158f, 0.316593379f,0.94856137f, 0.319502026f,0.947585583f, 0.322407693f,0.946600914f,
0.32531029f,0.945607305f, 0.328209847f,0.944604814f, 0.331106305f,0.943593442f, 0.333999664f,0.94257319f,
0.336889863f,0.941544056f, 0.339776874f,0.940506041f, 0.342660725f,0.939459205f, 0.345541328f,0.938403547f,
0.348418683f,0.937339008f, 0.351292759f,0.936265647f, 0.354163527f,0.935183525f, 0.357030958f,0.934092522f,
0.359895051f,0.932992816f, 0.362755716f,0.931884289f, 0.365612984f,0.93076694f, 0.368466824f,0.929640889f,
0.371317208f,0.928506076f, 0.374164075f,0.927362502f, 0.377007425f,0.926210225f, 0.379847199f,0.925049245f,
0.382683426f,0.923879504f, 0.385516047f,0.92270112f, 0.388345033f,0.921514034f, 0.391170382f,0.920318305f,
0.393992037f,0.919113874f, 0.396809995f,0.917900801f, 0.399624199f,0.916679084f, 0.402434647f,0.915448725f,
0.405241311f,0.914209783f, 0.408044159f,0.912962198f, 0.410843164f,0.91170603f, 0.413638324f,0.910441279f,
0.416429549f,0.909168005f, 0.419216901f,0.907886088f, 0.422000259f,0.906595707f, 0.424779683f,0.905296743f,
0.427555084f,0.903989315f, 0.430326492f,0.902673304f, 0.433093816f,0.901348829f, 0.435857087f,0.900015891f,
0.438616246f,0.898674488f, 0.441371262f,0.897324562f, 0.444122136f,0.895966232f, 0.446868837f,0.894599497f,
0.449611336f,0.893224299f, 0.452349573f,0.891840696f, 0.455083579f,0.890448749f, 0.457813293f,0.889048338f,
0.460538715f,0.887639642f, 0.463259786f,0.886222541f, 0.465976506f,0.884797096f, 0.468688816f,0.883363366f,
0.471396744f,0.881921291f, 0.474100202f,0.880470872f, 0.47679922f,0.879012227f, 0.479493767f,0.877545297f,
0.482183784f,0.876070082f, 0.484869242f,0.874586642f, 0.487550169f,0.873094976f, 0.490226477f,0.871595085f,
0.492898196f,0.870086968f, 0.495565265f,0.868570685f, 0.498227656f,0.867046237f, 0.500885367f,0.865513623f,
0.50353837f,0.863972843f, 0.506186664f,0.862423956f, 0.50883013f,0.860866964f, 0.511468828f,0.859301805f,
0.514102757f,0.857728601f, 0.516731799f,0.856147349f, 0.519356012f,0.854557991f, 0.521975279f,0.852960587f,
0.524589658f,0.851355195f, 0.527199149f,0.849741757f, 0.529803634f,0.848120332f, 0.532403111f,0.84649092f,
0.534997642f,0.84485358f, 0.537587047f,0.843208253f, 0.540171444f,0.841554999f, 0.542750776f,0.839893818f,
0.545324981f,0.838224709f, 0.547894061f,0.836547732f, 0.550457954f,0.834862888f, 0.553016722f,0.833170176f,
0.555570245f,0.831469595f, 0.558118522f,0.829761207f, 0.560661554f,0.82804507f, 0.563199341f,0.826321065f,
0.565731823f,0.824589312f, 0.568258941f,0.82284981f, 0.570780754f,0.8211025f, 0.573297143f,0.819347501f,
0.575808167f,0.817584813f, 0.578313768f,0.815814435f, 0.580813944f,0.81403631f, 0.583308637f,0.812250614f,
0.585797846f,0.81045717f, 0.588281572f,0.808656156f, 0.590759695f,0.806847572f, 0.593232274f,0.805031359f,
0.59569931f,0.803207517f, 0.598160684f,0.801376164f, 0.600616455f,0.799537241f, 0.603066623f,0.797690868f,
0.605511069f,0.795836926f, 0.607949793f,0.793975472f, 0.610382795f,0.792106569f, 0.612810075f,0.790230215f,
0.615231574f,0.78834641f, 0.61764729f,0.786455214f, 0.620057225f,0.784556568f, 0.622461259f,0.78265059f,
0.624859512f,0.780737221f, 0.627251804f,0.778816521f, 0.629638255f,0.77688849f, 0.632018745f,0.774953127f,
0.634393275f,0.773010433f, 0.636761844f,0.771060526f, 0.639124453f,0.769103348f, 0.641481042f,0.767138898f,
0.643831551f,0.765167236f, 0.64617604f,0.763188422f, 0.64851439f,0.761202395f, 0.65084666f,0.759209216f,
0.653172851f,0.757208824f, 0.655492842f,0.755201399f, 0.657806695f,0.753186822f, 0.660114348f,0.751165152f,
0.662415802f,0.749136388f, 0.664710999f,0.747100592f, 0.666999936f,0.745057762f, 0.669282615f,0.743007958f,
0.671558976f,0.740951121f, 0.673829019f,0.73888731f, 0.676092684f,0.736816585f, 0.678350031f,0.734738886f,
0.680601001f,0.732654274f, 0.682845533f,0.730562747f, 0.685083687f,0.728464365f, 0.687315345f,0.726359129f,
0.689540565f,0.724247098f, 0.691759229f,0.722128212f, 0.693971455f,0.720002532f, 0.696177125f,0.717870057f,
0.698376238f,0.715730846f, 0.700568795f,0.71358484f, 0.702754736f,0.711432219f, 0.704934061f,0.709272802f,
0.707106769f,0.707106769f, 0.709272802f,0.704934061f, 0.711432219f,0.702754736f, 0.71358484f,0.700568795f,
0.715730846f,0.698376238f, 0.717870057f,0.696177125f, 0.720002532f,0.693971455f, 0.722128212f,0.691759229f,
0.724247098f,0.689540565f, 0.726359129f,0.687315345f, 0.728464365f,0.685083687f, 0.730562747f,0.682845533f,
0.732654274f,0.680601001f, 0.734738886f,0.678350031f, 0.736816585f,0.676092684f, 0.73888731f,0.673829019f,
0.740951121f,0.671558976f, 0.743007958f,0.669282615f, 0.745057762f,0.666999936f, 0.747100592f,0.664710999f,
0.749136388f,0.662415802f, 0.751165152f,0.660114348f, 0.753186822f,0.657806695f, 0.755201399f,0.655492842f,
0.757208824f,0.653172851f, 0.759209216f,0.65084666f, 0.761202395f,0.64851439f, 0.763188422f,0.64617604f,
0.765167236f,0.643831551f, 0.767138898f,0.641481042f, 0.769103348f,0.639124453f, 0.771060526f,0.636761844f,
0.773010433f,0.634393275f, 0.774953127f,0.632018745f, 0.77688849f,0.629638255f, 0.778816521f,0.627251804f,
0.780737221f,0.624859512f, 0.78265059f,0.622461259f, 0.784556568f,0.620057225f, 0.786455214f,0.61764729f,
0.78834641f,0.615231574f, 0.790230215f,0.612810075f, 0.792106569f,0.610382795f, 0.793975472f,0.607949793f,
0.795836926f,0.605511069f, 0.797690868f,0.603066623f, 0.799537241f,0.600616455f, 0.801376164f,0.598160684f,
0.803207517f,0.59569931f, 0.805031359f,0.593232274f, 0.806847572f,0.590759695f, 0.808656156f,0.588281572f,
0.81045717f,0.585797846f, 0.812250614f,0.583308637f, 0.81403631f,0.580813944f, 0.815814435f,0.578313768f,
0.817584813f,0.575808167f, 0.819347501f,0.573297143f, 0.8211025f,0.570780754f, 0.82284981f,0.568258941f,
0.824589312f,0.565731823f, 0.826321065f,0.563199341f, 0.82804507f,0.560661554f, 0.829761207f,0.558118522f,
0.831469595f,0.555570245f, 0.833170176f,0.553016722f, 0.834862888f,0.550457954f, 0.836547732f,0.547894061f,
0.838224709f,0.545324981f, 0.839893818f,0.542750776f, 0.841554999f,0.540171444f, 0.843208253f,0.537587047f,
0.84485358f,0.534997642f, 0.84649092f,0.532403111f, 0.848120332f,0.529803634f, 0.849741757f,0.527199149f,
0.851355195f,0.524589658f, 0.852960587f,0.521975279f, 0.854557991f,0.519356012f, 0.856147349f,0.516731799f,
0.857728601f,0.514102757f, 0.859301805f,0.511468828f, 0.860866964f,0.50883013f, 0.862423956f,0.506186664f,
0.863972843f,0.50353837f, 0.865513623f,0.500885367f, 0.867046237f,0.498227656f, 0.868570685f,0.495565265f,
0.870086968f,0.492898196f, 0.871595085f,0.490226477f, 0.873094976f,0.487550169f, 0.874586642f,0.484869242f,
0.876070082f,0.482183784f, 0.877545297f,0.479493767f, 0.879012227f,0.47679922f, 0.880470872f,0.474100202f,
0.881921291f,0.471396744f, 0.883363366f,0.468688816f, 0.884797096f,0.465976506f, 0.886222541f,0.463259786f,
0.887639642f,0.460538715f, 0.889048338f,0.457813293f, 0.890448749f,0.455083579f, 0.891840696f,0.452349573f,
0.893224299f,0.449611336f, 0.894599497f,0.446868837f, 0.895966232f,0.444122136f, 0.897324562f,0.441371262f,
0.898674488f,0.438616246f, 0.900015891f,0.435857087f, 0.901348829f,0.433093816f, 0.902673304f,0.430326492f,
0.903989315f,0.427555084f, 0.905296743f,0.424779683f, 0.906595707f,0.422000259f, 0.907886088f,0.419216901f,
0.909168005f,0.416429549f, 0.910441279f,0.413638324f, 0.91170603f,0.410843164f, 0.912962198f,0.408044159f,
0.914209783f,0.405241311f, 0.915448725f,0.402434647f, 0.916679084f,0.399624199f, 0.917900801f,0.396809995f,
0.919113874f,0.393992037f, 0.920318305f,0.391170382f, 0.921514034f,0.388345033f, 0.92270112f,0.385516047f,
0.923879504f,0.382683426f, 0.925049245f,0.379847199f, 0.926210225f,0.377007425f, 0.927362502f,0.374164075f,
0.928506076f,0.371317208f, 0.929640889f,0.368466824f, 0.93076694f,0.365612984f, 0.931884289f,0.362755716f,
0.932992816f,0.359895051f, 0.934092522f,0.357030958f, 0.935183525f,0.354163527f, 0.936265647f,0.351292759f,
0.937339008f,0.348418683f, 0.938403547f,0.345541328f, 0.939459205f,0.342660725f, 0.940506041f,0.339776874f,
0.941544056f,0.336889863f, 0.94257319f,0.333999664f, 0.943593442f,0.331106305f, 0.944604814f,0.328209847f,
0.945607305f,0.32531029f, 0.946600914f,0.322407693f, 0.947585583f,0.319502026f, 0.94856137f,0.316593379f,
0.949528158f,0.313681751f, 0.950486064f,0.310767144f, 0.95143503f,0.307849646f, 0.952374995f,0.304929227f,
0.953306019f,0.302005947f, 0.954228103f,0.299079835f, 0.955141187f,0.296150893f, 0.95604527f,0.293219149f,
0.956940353f,0.290284663f, 0.957826436f,0.287347466f, 0.958703458f,0.284407526f, 0.95957154f,0.281464934f,
0.960430503f,0.27851969f, 0.961280465f,0.275571823f, 0.962121427f,0.272621363f, 0.962953269f,0.269668311f,
0.963776052f,0.266712755f, 0.964589775f,0.263754666f, 0.965394437f,0.260794103f, 0.966189981f,0.257831097f,
0.966976464f,0.254865646f, 0.967753828f,0.251897812f, 0.968522072f,0.248927608f, 0.969281256f,0.24595505f,
0.970031261f,0.242980182f, 0.970772147f,0.24000302f, 0.971503913f,0.237023607f, 0.972226501f,0.234041959f,
0.972939968f,0.231058106f, 0.973644257f,0.228072077f, 0.974339366f,0.225083917f, 0.975025356f,0.222093627f,
0.975702107f,0.219101235f, 0.976369739f,0.216106802f, 0.977028131f,0.213110313f, 0.977677345f,0.210111842f,
0.97831738f,0.207111374f, 0.978948176f,0.204108968f, 0.979569793f,0.201104641f, 0.980182111f,0.198098406f,
0.980785251f,0.195090324f, 0.981379211f,0.192080393f, 0.981963873f,0.18906866f, 0.982539296f,0.186055154f,
0.983105481f,0.183039889f, 0.983662426f,0.180022895f, 0.984210074f,0.177004218f, 0.984748483f,0.173983872f,
0.985277653f,0.170961887f, 0.985797524f,0.167938292f, 0.986308098f,0.164913118f, 0.986809373f,0.161886394f,
0.987301409f,0.15885815f, 0.987784147f,0.155828401f, 0.988257587f,0.152797192f, 0.988721669f,0.149764538f,
0.989176512f,0.146730468f, 0.989621997f,0.143695027f, 0.990058184f,0.140658244f, 0.990485072f,0.137620121f,
0.990902662f,0.134580702f, 0.991310835f,0.13154003f, 0.991709769f,0.128498107f, 0.992099285f,0.125454977f,
0.992479563f,0.122410677f, 0.992850423f,0.119365215f, 0.993211925f,0.116318628f, 0.993564129f,0.113270953f,
0.993906975f,0.110222206f, 0.994240463f,0.107172422f, 0.994564593f,0.104121633f, 0.994879305f,0.10106986f,
0.99518472f,0.0980171412f, 0.995480776f,0.0949634984f, 0.995767415f,0.0919089541f, 0.996044695f,0.0888535529f,
0.996312618f,0.0857973099f, 0.996571124f,0.0827402622f, 0.996820271f,0.0796824396f, 0.997060061f,0.0766238645f,
0.997290432f,0.0735645667f, 0.997511446f,0.070504576f, 0.997723043f,0.0674439222f, 0.997925282f,0.0643826276f,
0.998118103f,0.061320737f, 0.998301566f,0.0582582653f, 0.998475552f,0.0551952459f, 0.998640239f,0.052131705f,
0.99879545f,0.0490676761f, 0.998941302f,0.0460031815f, 0.999077737f,0.0429382585f, 0.999204755f,0.0398729257f,
0.999322355f,0.0368072242f, 0.999430597f,0.0337411724f, 0.999529421f,0.030674804f, 0.999618828f,0.027608145f,
0.999698818f,0.024541229f, 0.99976939f,0.0214740802f, 0.999830604f,0.0184067301f, 0.99988234f,0.015339206f,
0.999924719f,0.0122715384f, 0.999957621f,0.00920375437f, 0.999981165f,0.00613588467f, 0.999995291f,0.00306795677f,
};

#define STAMPK(kid, i)
#define STAMP(i) STAMPK(0, i)
struct cf { float x, y; };
__device__ __forceinline__ cf tw(int i) { const float2 v = *reinterpret_cast<const float2*>(&TWF[2 * i]); return cf{v.x, v.y}; }

__device__ __forceinline__ float bf2f(uint32_t u16) { return __uint_as_float(u16 << 16); }
__device__ __forceinline__ uint32_t f2bf(float f) { return (uint32_t)__bfloat16_as_ushort(__float2bfloat16(f)); }
__device__ __forceinline__ float bfr(float f) { return bf2f(f2bf(f)); }

__device__ __forceinline__ cf cadd(cf a, cf b) { return cf{a.x + b.x, a.y + b.y}; }
__device__ __forceinline__ cf csub(cf a, cf b) { return cf{a.x - b.x, a.y - b.y}; }
__device__ __forceinline__ cf cmul(cf a, cf b) { return cf{a.x * b.x - a.y * b.y, a.x * b.y + a.y * b.x}; }
__device__ __forceinline__ cf cmulc(cf a, cf b) { return cf{a.x * b.x + a.y * b.y, a.y * b.x - a.x * b.y}; }
template <int S> __device__ __forceinline__ cf twmul(cf a, cf w) { return S < 0 ? cmul(a, w) : cmulc(a, w); }
template <int S> __device__ __forceinline__ cf rot(cf a) { return S > 0 ? cf{-a.y, a.x} : cf{a.y, -a.x}; }

template <int S> __device__ __forceinline__ void dft4(cf& a0, cf& a1, cf& a2, cf& a3) {
  const cf t0 = cadd(a0, a2), t1 = csub(a0, a2), t2 = cadd(a1, a3), t3 = rot<S>(csub(a1, a3));
  a0 = cadd(t0, t2); a1 = cadd(t1, t3); a2 = csub(t0, t2); a3 = csub(t1, t3);
}
template <int S> __device__ __forceinline__ void dft8(cf (&a)[8]) {
  constexpr float C = 0.70710678118654752440f;
  cf u0 = cadd(a[0], a[4]), v0 = csub(a[0], a[4]);
  cf u1 = cadd(a[1], a[5]), v1 = csub(a[1], a[5]);
  cf u2 = cadd(a[2], a[6]), v2 = csub(a[2], a[6]);
  cf u3 = cadd(a[3], a[7]), v3 = csub(a[3], a[7]);
  v1 = cf{C * (v1.x - S * v1.y), C * (v1.y + S * v1.x)};
  v2 = rot<S>(v2);
  v3 = cf{C * (-v3.x - S * v3.y), C * (S * v3.x - v3.y)};
  dft4<S>(u0, u1, u2, u3);
  dft4<S>(v0, v1, v2, v3);
  a[0] = u0; a[2] = u1; a[4] = u2; a[6] = u3;
  a[1] = v0; a[3] = v1; a[5] = v2; a[7] = v3;
}

template <int CTRL> __device__ __forceinline__ float dpp_add(float v) {
  return v + __uint_as_float(__builtin_amdgcn_update_dpp(0u, __float_as_uint(v), CTRL, 0xf, 0xf, false));
}
__device__ __forceinline__ float half_sum(float v) {
  v = dpp_add<0xB1>(v);
  v = dpp_add<0x4E>(v);
  v = dpp_add<0x124>(v);
  v = dpp_add<0x128>(v);
  return v + __shfl_xor(v, 16);
}
__device__ __forceinline__ int swz(int n) { return n ^ ((n >> 4) & 15); }
constexpr int ZRS = 260;

__device__ __forceinline__ void wsync() {
  __builtin_amdgcn_fence(__ATOMIC_RELEASE, "wavefront");
  __builtin_amdgcn_wave_barrier();
  __builtin_amdgcn_fence(__ATOMIC_ACQUIRE, "wavefront");
}
template <int S> __device__ __forceinline__ void twiddle8(cf (&a)[8], cf w) {
  const cf w2 = cmul(w, w), w3 = cmul(w2, w), w4 = cmul(w2, w2), w5 = cmul(w4, w), w6 = cmul(w3, w3), w7 = cmul(w4, w3);
  a[1] = twmul<S>(a[1], w); a[2] = twmul<S>(a[2], w2); a[3] = twmul<S>(a[3], w3); a[4] = twmul<S>(a[4], w4);
  a[5] = twmul<S>(a[5], w5); a[6] = twmul<S>(a[6], w6); a[7] = twmul<S>(a[7], w7);
}
template <int S> __device__ __forceinline__ void twiddle4(cf (&a)[4], cf w) {
  const cf w2 = cmul(w, w), w3 = cmul(w2, w);
  a[1] = twmul<S>(a[1], w); a[2] = twmul<S>(a[2], w2); a[3] = twmul<S>(a[3], w3);
}

template <int S> __device__ __forceinline__ void fft256_row(cf (&a)[8], cf (&res)[2][4], cf* zrow, int t, cf w2, const cf (&w3)[2]) {
  dft8<S>(a);
#pragma unroll
  for (int j = 0; j < 8; ++j) zrow[swz(8 * t + j)] = a[j];
  wsync();
#pragma unroll
  for (int j = 0; j < 8; ++j) a[j] = zrow[swz(t + 32 * j)];
  {
    const int k = t & 7;
    twiddle8<S>(a, w2);
    dft8<S>(a);
    wsync();
#pragma unroll
    for (int j = 0; j < 8; ++j) zrow[swz((t - k) * 8 + k + 8 * j)] = a[j];
  }
  wsync();
#pragma unroll
  for (int h = 0; h < 2; ++h) {
    const int i = t + 32 * h;
#pragma unroll
    for (int j = 0; j < 4; ++j) res[h][j] = zrow[swz(i + 64 * j)];
    twiddle4<S>(res[h], w3[h]);
    dft4<S>(res[h][0], res[h][1], res[h][2], res[h][3]);
  }
}

__global__ __launch_bounds__(256) void k1_ln_rfft(const uint16_t* __restrict__ x, const uint16_t* __restrict__ wr,
                                                  const uint16_t* __restrict__ wi, uint16_t* __restrict__ xn,
                                                  float2* __restrict__ X1T, uint32_t* __restrict__ WT,
                                                  const uint16_t* __restrict__ wg, const uint16_t* __restrict__ wu,
                                                  const uint16_t* __restrict__ wd) {
  __shared__ cf zs[8 * ZRS];
  const int tid = threadIdx.x, s0 = blockIdx.x * 8;
  const int r = tid >> 5, t = tid & 31;
  const int r2 = tid & 7, kb = tid >> 3;
  const int row = s0 + r;
  const uint32_t* xrow = reinterpret_cast<const uint32_t*>(x + row * DM);
  STAMPK(1, 0);
  uint32_t xu[8];
#pragma unroll
  for (int j = 0; j < 8; ++j) xu[j] = xrow[t + 32 * j];
  const cf wp2 = tw(32 * (t & 7));
  const cf wp3[2] = {tw(8 * t), tw(8 * (t + 32))};
  cf wsp[4];
#pragma unroll
  for (int it = 0; it < 4; ++it) wsp[it] = tw(4 * (kb + 32 * it));
  uint32_t wv[9];
#pragma unroll
  for (int it = 0; it < 9; ++it) {
    const int c = kb + 32 * it;
    wv[it] = 0;
    if (c < NF) wv[it] = (uint32_t)wr[(s0 + r2) * NF + c] | ((uint32_t)wi[(s0 + r2) * NF + c] << 16);
  }
  float v[16];
#pragma unroll
  for (int j = 0; j < 8; ++j) { v[2 * j] = bf2f(xu[j] & 0xffffu); v[2 * j + 1] = bf2f(xu[j] >> 16); }
  float sum = 0.f;
#pragma unroll
  for (int j = 0; j < 16; ++j) sum += v[j];
  sum = half_sum(sum);
  const float mean = sum * (1.0f / 512.0f);
  float sq = 0.f;
#pragma unroll
  for (int j = 0; j < 16; ++j) { const float d = v[j] - mean; sq += d * d; }
  sq = half_sum(sq);
  STAMPK(1, 1);
  const float m_bf = bfr(mean);
  const float v_bf = bfr(sq * (1.0f / 512.0f));
  const float den = bfr(sqrtf(bfr(v_bf + 1e-5f)));
  const float rden = 1.0f / den;
  cf a[8];
  uint32_t* xnrow = reinterpret_cast<uint32_t*>(xn + row * DM);
#pragma unroll
  for (int j = 0; j < 8; ++j) {
    const float d0 = bfr(v[2 * j] - m_bf), d1 = bfr(v[2 * j + 1] - m_bf);
    float q0 = d0 * rden, q1 = d1 * rden;
    q0 = fmaf(fmaf(-q0, den, d0), rden, q0); q1 = fmaf(fmaf(-q1, den, d1), rden, q1);
    const uint32_t b0 = f2bf(q0), b1 = f2bf(q1);
    xnrow[t + 32 * j] = b0 | (b1 << 16);
    a[j] = cf{bf2f(b0), bf2f(b1)};
  }
  STAMPK(1, 2);
#pragma unroll
  for (int it = 0; it < 9; ++it) {
    const int c = kb + 32 * it;
    if (c < NF) WT[c * SEQ + s0 + r2] = wv[it];
  }
  cf res[2][4];
  cf* zrow = zs + r * ZRS;
  STAMPK(1, 3);
  fft256_row<-1>(a, res, zrow, t, wp2, wp3);
  STAMPK(1, 4);
  wsync();
#pragma unroll
  for (int h = 0; h < 2; ++h)
#pragma unroll
    for (int j = 0; j < 4; ++j) zrow[swz(t + 32 * h + 64 * j)] = res[h][j];
  __syncthreads();
  {
    const cf* zr = zs + r2 * ZRS;
#pragma unroll
    for (int it = 0; it < 4; ++it) {
      const int k = kb + 32 * it;
      const cf A = zr[swz(k)];
      cf B = zr[swz((256 - k) & 255)]; B.y = -B.y;
      const cf E = cf{0.5f * (A.x + B.x), 0.5f * (A.y + B.y)};
      const cf O = cf{0.5f * (A.y - B.y), -0.5f * (A.x - B.x)};
      const cf T = cmul(wsp[it], O);
      X1T[k * SEQ + s0 + r2] = make_float2(E.x + T.x, E.y + T.y);
      X1T[(256 - k) * SEQ + s0 + r2] = make_float2(E.x - T.x, -(E.y - T.y));
    }
    if (kb == 0) {
      const cf A = zr[swz(128)];
      X1T[128 * SEQ + s0 + r2] = make_float2(A.x, -A.y);
    }
  }
  {
    const int gt = blockIdx.x * 256 + tid;
    const u32x4* p0 = reinterpret_cast<const u32x4*>(wg);
    const u32x4* p1 = reinterpret_cast<const u32x4*>(wu);
    const u32x4* p2 = reinterpret_cast<const u32x4*>(wd);
    u32x4 acc = p0[gt] ^ p1[gt] ^ p2[gt];
    if (gt + 65536 < 114688) acc ^= p0[gt + 65536] ^ p1[gt + 65536] ^ p2[gt + 65536];
    asm volatile("" :: "v"(acc));
  }
  STAMPK(1, 5);
}

__device__ __forceinline__ void k2_body(int col, const float2* __restrict__ X1T, const uint32_t* __restrict__ WT,
                                        float2* __restrict__ Z2T, char* smem) {
  cf (*zs)[2048] = reinterpret_cast<cf (*)[2048]>(smem);
  const int t = threadIdx.x;
  STAMPK(3, 0);
  const bool special = (col == 0);
  const float2* xin = X1T + col * SEQ;
  cf b4[2][4];
#pragma unroll
  for (int h = 0; h < 2; ++h)
#pragma unroll
    for (int j = 0; j < 4; ++j) {
      const float2 v = xin[t + 256 * h + 512 * j];
      b4[h][j] = cf{v.x, v.y};
      if (special) b4[h][j].y = X1T[256 * SEQ + t + 256 * h + 512 * j].x;
    }
  const cf wB = tw(64 * (t & 3)), wC = tw(8 * (t & 31)), wD = tw(t);
  const cf wF = tw(32 * (t & 7)), wG = tw(4 * (t & 63));
  const cf wH[2] = {tw(t), tw(t + 256)};
  uint32_t wv[8];
#pragma unroll
  for (int j = 0; j < 8; ++j) wv[j] = WT[col * SEQ + t + 256 * j];
#pragma unroll
  for (int h = 0; h < 2; ++h) {
    dft4<-1>(b4[h][0], b4[h][1], b4[h][2], b4[h][3]);
#pragma unroll
    for (int j = 0; j < 4; ++j) zs[0][swz(4 * (t + 256 * h) + j)] = b4[h][j];
  }
  __syncthreads();
  STAMPK(3, 1);
  cf a[8];
  {
#pragma unroll
    for (int j = 0; j < 8; ++j) a[j] = zs[0][swz(t + 256 * j)];
    const int k = t & 3;
    twiddle8<-1>(a, wB);
    dft8<-1>(a);
#pragma unroll
    for (int j = 0; j < 8; ++j) zs[1][swz((t - k) * 8 + k + 4 * j)] = a[j];
    __syncthreads();
  }
  {
#pragma unroll
    for (int j = 0; j < 8; ++j) a[j] = zs[1][swz(t + 256 * j)];
    const int k = t & 31;
    twiddle8<-1>(a, wC);
    dft8<-1>(a);
#pragma unroll
    for (int j = 0; j < 8; ++j) zs[0][swz((t - k) * 8 + k + 32 * j)] = a[j];
    __syncthreads();
  }
  {
#pragma unroll
    for (int j = 0; j < 8; ++j) a[j] = zs[0][swz(t + 256 * j)];
    twiddle8<-1>(a, wD);
    dft8<-1>(a);
  }
  STAMPK(3, 2);
  if (!special) {
#pragma unroll
    for (int j = 0; j < 8; ++j) {
      const cf w = cf{bf2f(wv[j] & 0xffffu) * (1.0f / 2048.0f), bf2f(wv[j] >> 16) * (1.0f / 2048.0f)};
      a[j] = cmul(a[j], w);
    }
  } else {
#pragma unroll
    for (int j = 0; j < 8; ++j) zs[1][swz(t + 256 * j)] = a[j];
    __syncthreads();
#pragma unroll
    for (int j = 0; j < 8; ++j) {
      const int f = t + 256 * j, fm = (2048 - f) & 2047;
      const cf zm = zs[1][swz(fm)];
      const uint32_t wam = WT[fm], wb = WT[256 * SEQ + f], wbm = WT[256 * SEQ + fm];
      const cf A = cf{0.5f * (a[j].x + zm.x), 0.5f * (a[j].y - zm.y)};
      const cf B = cf{0.5f * (a[j].y + zm.y), -0.5f * (a[j].x - zm.x)};
      const cf wsa = cf{(bf2f(wv[j] & 0xffffu) + bf2f(wam & 0xffffu)) * (0.5f / 2048.0f), (bf2f(wv[j] >> 16) - bf2f(wam >> 16)) * (0.5f / 2048.0f)};
      const cf wsb = cf{(bf2f(wb & 0xffffu) + bf2f(wbm & 0xffffu)) * (0.5f / 2048.0f), (bf2f(wb >> 16) - bf2f(wbm >> 16)) * (0.5f / 2048.0f)};
      const cf ya = cmul(A, wsa), yb = cmul(B, wsb);
      a[j] = cf{ya.x - yb.y, ya.y + yb.x};
    }
    __syncthreads();
  }
  {
    dft8<1>(a);
#pragma unroll
    for (int j = 0; j < 8; ++j) zs[1][swz(8 * t + j)] = a[j];
    __syncthreads();
  }
  {
#pragma unroll
    for (int j = 0; j < 8; ++j) a[j] = zs[1][swz(t + 256 * j)];
    const int k = t & 7;
    twiddle8<1>(a, wF);
    dft8<1>(a);
#pragma unroll
    for (int j = 0; j < 8; ++j) zs[0][swz((t - k) * 8 + k + 8 * j)] = a[j];
    __syncthreads();
  }
  {
#pragma unroll
    for (int j = 0; j < 8; ++j) a[j] = zs[0][swz(t + 256 * j)];
    const int k = t & 63;
    twiddle8<1>(a, wG);
    dft8<1>(a);
#pragma unroll
    for (int j = 0; j < 8; ++j) zs[1][swz((t - k) * 8 + k + 64 * j)] = a[j];
    __syncthreads();
  }
  STAMPK(3, 3);
  float2* zout = Z2T + col * SEQ;
#pragma unroll
  for (int h = 0; h < 2; ++h) {
    const int i = t + 256 * h;
#pragma unroll
    for (int j = 0; j < 4; ++j) b4[h][j] = zs[1][swz(i + 512 * j)];
    twiddle4<1>(b4[h], wH[h]);
    dft4<1>(b4[h][0], b4[h][1], b4[h][2], b4[h][3]);
#pragma unroll
    for (int j = 0; j < 4; ++j) {
      if (!special) zout[i + 512 * j] = make_float2(b4[h][j].x, b4[h][j].y);
      else { zout[i + 512 * j] = make_float2(b4[h][j].x, 0.f); Z2T[256 * SEQ + i + 512 * j] = make_float2(b4[h][j].y, 0.f); }
    }
  }
  STAMPK(3, 4);
}

__device__ __forceinline__ s16x4 tr_read(const char* lds_ptr) {
  typedef __attribute__((address_space(3))) s16x4* lp;
  return __builtin_amdgcn_ds_read_tr16_b64_v4i16((lp)(lds_ptr));
}

__device__ __forceinline__ void glds16(const void* gsrc, uint32_t lds_dst) {
  uint32_t keep;
  asm volatile("s_mov_b32 %0, m0\n\ts_mov_b32 m0, %2\n\ts_nop 0\n\tglobal_load_lds_dwordx4 %1, off\n\ts_mov_b32 m0, %0"
               : "=&s"(keep) : "v"(gsrc), "s"(lds_dst) : "memory");
}
template <int N> __device__ __forceinline__ void wait_vm() { asm volatile("s_waitcnt vmcnt(%0)" :: "n"(N) : "memory"); }
__device__ __forceinline__ uint32_t lds_addr_of(const void* p) {
  return (uint32_t)(size_t)(__attribute__((address_space(3))) const char*)p;
}

__device__ __forceinline__ float silu(float a) {
  return a * __builtin_amdgcn_rcpf(1.0f + __builtin_amdgcn_exp2f(-1.4426950408889634f * a));
}

constexpr int G1_STAGE = 48 * 1024;
typedef __attribute__((ext_vector_type(2))) float f32x2;
__device__ __forceinline__ void mfma16(f32x4& c, const bf16x8& a, const bf16x8& b) {
  c = __builtin_amdgcn_mfma_f32_16x16x32_bf16(a, b, c, 0, 0, 0);
}
__device__ __forceinline__ void mfma_drain() { asm volatile("s_nop 7\n\ts_nop 7\n\ts_nop 7" ::: "memory"); }
__device__ __forceinline__ f32x2 bfr2(f32x2 v) { return f32x2{bfr(v[0]), bfr(v[1])}; }
__device__ __forceinline__ uint32_t pk2bf(f32x2 v) { return f2bf(v[0]) | (f2bf(v[1]) << 16); }
__device__ __forceinline__ f32x2 silu2(f32x2 a) {
  const f32x2 t = a * -1.4426950408889634f;
  const f32x2 e = f32x2{__builtin_amdgcn_exp2f(t[0]), __builtin_amdgcn_exp2f(t[1])} + 1.0f;
  return a * f32x2{__builtin_amdgcn_rcpf(e[0]), __builtin_amdgcn_rcpf(e[1])};
}

struct G1Frag { bf16x8 a[4]; bf16x8 b[2][2]; };
__device__ __forceinline__ void g1_load(G1Frag& f, const char* buf, int ks, int wm, int wn, int lane) {
  const int g = lane >> 4, q = (lane & 15) >> 2, p = lane & 3;
#pragma unroll
  for (int mt = 0; mt < 4; ++mt) {
    const int row = wm * 64 + mt * 16 + (lane & 15), ch = ks * 4 + (lane >> 4);
    f.a[mt] = *reinterpret_cast<const bf16x8*>(buf + row * 128 + ((ch ^ ((row >> 1) & 7)) << 4));
  }
  const int sw = q | ((g & 1) << 2);
#pragma unroll
  for (int mat = 0; mat < 2; ++mat)
#pragma unroll
    for (int nt = 0; nt < 2; ++nt) {
      const int cc = wn * 2 + nt;
      const char* bb = buf + 16384 + mat * 16384 + ((cc ^ sw) << 5) + 8 * p;
      const s16x4 lo = tr_read(bb + (ks * 32 + 8 * g + q) * 256);
      const s16x4 hi = tr_read(bb + (ks * 32 + 8 * g + 4 + q) * 256);
      f.b[mat][nt] = bf16x8{lo[0], lo[1], lo[2], lo[3], hi[0], hi[1], hi[2], hi[3]};
    }
}
__device__ __forceinline__ void g1_mma(f32x4 (&acc)[2][4][2], const G1Frag& f) {
#pragma unroll
  for (int mat = 0; mat < 2; ++mat)
#pragma unroll
    for (int nt = 0; nt < 2; ++nt)
#pragma unroll
      for (int mt = 0; mt < 4; ++mt) mfma16(acc[mat][mt][nt], f.b[mat][nt], f.a[mt]);
}
template <class F> __device__ __forceinline__ void g1_mma_i(f32x4 (&acc)[2][4][2], const G1Frag& f, F&& hook) {
#pragma unroll
  for (int mat = 0; mat < 2; ++mat)
#pragma unroll
    for (int nt = 0; nt < 2; ++nt)
#pragma unroll
      for (int mt = 0; mt < 4; ++mt) {
        mfma16(acc[mat][mt][nt], f.b[mat][nt], f.a[mt]);
        const int c = mat * 8 + nt * 4 + mt;
        if (c == 3) hook(0);
        if (c == 8) hook(1);
        if (c == 13) hook(2);
      }
}
__device__ __forceinline__ void g1_body(int vb, const uint16_t* __restrict__ xn, const uint16_t* __restrict__ Wg,
                                        const uint16_t* __restrict__ Wu, uint16_t* __restrict__ H, char* smem) {
  const int tid = threadIdx.x, lane = tid & 63, wid = tid >> 6;
  const int xcd = vb & 7, j8 = vb >> 3;
  const int bm = (xcd >> 1) * 4 + (j8 / 7), bn = (xcd & 1) * 7 + (j8 % 7);
  const int m0 = bm * 128, n0 = bn * 128;
  const int wm = wid >> 2, wn = wid & 3;

  const uint32_t ldsw = __builtin_amdgcn_readfirstlane(lds_addr_of(smem) + wid * 2048);
  auto piece = [&](int kt, int bufi, int i, int j) {
    const int qi = wid * 2 + i;
    const uint32_t dst = ldsw + bufi * G1_STAGE + i * 1024;
    if (j == 0) {
      const int r = 8 * qi + (lane >> 3), cp = lane & 7, ch = cp ^ ((r >> 1) & 7);
      glds16(xn + (m0 + r) * DM + kt * 64 + ch * 8, dst);
    } else {
      const int k = 4 * qi + (lane >> 4), ps = lane & 15, cc = (ps >> 1) ^ ((k & 3) | (((k >> 3) & 1) << 2)), half = ps & 1;
      const long off = (long)(kt * 64 + k) * FF + n0 + cc * 16 + half * 8;
      if (j == 1) glds16(Wg + off, dst + 16384); else glds16(Wu + off, dst + 32768);
    }
  };

  f32x4 acc[2][4][2];
#pragma unroll
  for (int a = 0; a < 2; ++a)
#pragma unroll
    for (int b = 0; b < 4; ++b)
#pragma unroll
      for (int c = 0; c < 2; ++c) acc[a][b][c] = f32x4{0.f, 0.f, 0.f, 0.f};

  G1Frag fx, fy;
  STAMP(0);
#pragma unroll
  for (int t = 0; t < 2; ++t)
#pragma unroll
    for (int i = 0; i < 2; ++i)
#pragma unroll
      for (int j = 0; j < 3; ++j) piece(t, t, i, j);
#pragma unroll
  for (int j = 0; j < 3; ++j) piece(2, 2, 0, j);
  STAMP(1);
  wait_vm<9>();
  __syncthreads();
  STAMP(2);
  g1_load(fx, smem, 0, wm, wn, lane);
  g1_load(fy, smem, 1, wm, wn, lane);
  int bufn = 1;
  for (int kt = 0; kt < 7; ++kt) {
    const int bufp = bufn == 2 ? 0 : bufn + 1;
    if (kt + 2 < 8) g1_mma_i(acc, fx, [&](int j) { piece(kt + 2, bufp, 1, j); });
    else g1_mma(acc, fx);
    if (kt < 6) wait_vm<6>(); else wait_vm<0>();
    __syncthreads();
    const char* buf = smem + bufn * G1_STAGE;
    g1_load(fx, buf, 0, wm, wn, lane);
    if (kt + 3 < 8) g1_mma_i(acc, fy, [&](int j) { piece(kt + 3, bufn == 0 ? 2 : bufn - 1, 0, j); });
    else g1_mma(acc, fy);
    g1_load(fy, buf, 1, wm, wn, lane);
    bufn = bufp;
  }
  STAMP(3);
  g1_mma(acc, fx);
  g1_mma(acc, fy);
  mfma_drain();
  STAMP(4);
#pragma unroll
  for (int mt = 0; mt < 4; ++mt)
#pragma unroll
    for (int nt = 0; nt < 2; ++nt) {
      const int m = m0 + wm * 64 + mt * 16 + (lane & 15);
      const int n = n0 + wn * 32 + nt * 16 + (lane >> 4) * 4;
      u32x2 o;
#pragma unroll
      for (int e = 0; e < 2; ++e) {
        const f32x2 ga = bfr2(f32x2{acc[0][mt][nt][2 * e], acc[0][mt][nt][2 * e + 1]});
        const f32x2 sg = bfr2(silu2(ga));
        const f32x2 ub = bfr2(f32x2{acc[1][mt][nt][2 * e], acc[1][mt][nt][2 * e + 1]});
        o[e] = pk2bf(sg * ub);
      }
      *reinterpret_cast<u32x2*>(H + (long)m * FF + n) = o;
    }
  STAMP(5);
}

__global__ __launch_bounds__(512, 2) void g1k2(const uint16_t* __restrict__ xn, const uint16_t* __restrict__ Wg,
                                               const uint16_t* __restrict__ Wu, uint16_t* __restrict__ H,
                                               const float2* __restrict__ X1T, const uint32_t* __restrict__ WT,
                                               float2* __restrict__ Z2T) {
  extern __shared__ __attribute__((aligned(16))) char smem[];
  if (blockIdx.x < 224) {
    g1_body(blockIdx.x, xn, Wg, Wu, H, smem);
  } else {
    if (threadIdx.x >= 256) return;
    k2_body(blockIdx.x - 224, X1T, WT, Z2T, smem);
  }
}

constexpr int G2_STAGE = 32 * 1024;
constexpr int G2_LDS = 4 * G2_STAGE;
struct G2Frag { bf16x8 a[4]; bf16x8 b[2]; };
__device__ __forceinline__ void g2_load(G2Frag& f, const char* buf, int wk, int wn, int lane) {
  const int g = lane >> 4, q = (lane & 15) >> 2, p = lane & 3;
#pragma unroll
  for (int mt = 0; mt < 4; ++mt) {
    const int row = mt * 16 + (lane & 15), ch = wk * 4 + (lane >> 4);
    f.a[mt] = *reinterpret_cast<const bf16x8*>(buf + row * 256 + ((ch ^ (row & 15)) << 4));
  }
  const int sw = (q >> 1) | ((g & 1) << 1);
#pragma unroll
  for (int nt = 0; nt < 2; ++nt) {
    const char* bb = buf + 16384 + (((wn * 2 + nt) ^ sw) << 5) + 8 * p;
    const s16x4 lo = tr_read(bb + (wk * 32 + 8 * g + q) * 128);
    const s16x4 hi = tr_read(bb + (wk * 32 + 8 * g + 4 + q) * 128);
    f.b[nt] = bf16x8{lo[0], lo[1], lo[2], lo[3], hi[0], hi[1], hi[2], hi[3]};
  }
}
__device__ __forceinline__ void g2_mma(f32x4 (&acc)[4][2], const G2Frag& f) {
#pragma unroll
  for (int nt = 0; nt < 2; ++nt)
#pragma unroll
    for (int mt = 0; mt < 4; ++mt) mfma16(acc[mt][nt], f.b[nt], f.a[mt]);
}
__global__ __launch_bounds__(512, 2) void g2_gemm(const uint16_t* __restrict__ H, const uint16_t* __restrict__ Wd,
                                                  uint16_t* __restrict__ G) {
  extern __shared__ __attribute__((aligned(16))) char smem[];
  const int tid = threadIdx.x, lane = tid & 63, wid = tid >> 6;
  const int xcd = blockIdx.x & 7, j8 = blockIdx.x >> 3;
  const int bm = xcd * 4 + (j8 >> 3), bn = j8 & 7;
  const int m0 = bm * 64, n0 = bn * 64;
  const int wk = wid & 3, wn = wid >> 2;

  const uint32_t ldsw = __builtin_amdgcn_readfirstlane(lds_addr_of(smem) + wid * 2048);
  auto stage = [&](int kt, int bufi) {
#pragma unroll
    for (int i = 0; i < 2; ++i) {
      const int qi = wid * 2 + i;
      const uint32_t dst = ldsw + bufi * G2_STAGE + i * 1024;
      { const int r = 4 * qi + (lane >> 4), cp = lane & 15, ch = cp ^ (r & 15);
        glds16(H + (long)(m0 + r) * FF + kt * 128 + ch * 8, dst); }
      { const int k = 8 * qi + (lane >> 3), ps = lane & 7, cc = (ps >> 1) ^ (((k >> 1) & 1) | (((k >> 3) & 1) << 1)), half = ps & 1;
        glds16(Wd + (long)(kt * 128 + k) * DM + n0 + cc * 16 + half * 8, dst + 16384); }
    }
  };

  f32x4 acc[4][2];
#pragma unroll
  for (int b = 0; b < 4; ++b)
#pragma unroll
    for (int c = 0; c < 2; ++c) acc[b][c] = f32x4{0.f, 0.f, 0.f, 0.f};

  G2Frag fx, fy;
  STAMPK(2, 0);
  stage(0, 0);
  stage(1, 1);
  stage(2, 2);
  stage(3, 3);
  STAMPK(2, 1);
  wait_vm<12>();
  __syncthreads();
  STAMPK(2, 2);
  g2_load(fx, smem, wk, wn, lane);
#pragma unroll 1
  for (int kt = 0; kt < 14; kt += 2) {
    if (kt + 1 <= 11) wait_vm<8>(); else if (kt + 1 == 12) wait_vm<4>(); else wait_vm<0>();
    __syncthreads();
    if (kt + 4 < 14) stage(kt + 4, kt & 3);
    g2_load(fy, smem + ((kt + 1) & 3) * G2_STAGE, wk, wn, lane);
    g2_mma(acc, fx);
    if (kt + 2 < 14) {
      if (kt + 2 <= 11) wait_vm<8>(); else wait_vm<4>();
      __syncthreads();
      if (kt + 5 < 14) stage(kt + 5, (kt + 1) & 3);
      g2_load(fx, smem + ((kt + 2) & 3) * G2_STAGE, wk, wn, lane);
    }
    g2_mma(acc, fy);
  }
  STAMPK(2, 3);
  mfma_drain();
  __syncthreads();
  float* P = reinterpret_cast<float*>(smem);
#pragma unroll
  for (int mt = 0; mt < 4; ++mt)
#pragma unroll
    for (int nt = 0; nt < 2; ++nt) {
      const int m = mt * 16 + (lane & 15), n = wn * 32 + nt * 16 + (lane >> 4) * 4;
      *reinterpret_cast<f32x4*>(P + wk * (64 * 68) + m * 68 + n) = acc[mt][nt];
    }
  __syncthreads();
  {
    const int m = wid * 8 + (lane >> 3), nb = (lane & 7) * 8;
    uint32_t ob[4];
#pragma unroll
    for (int c = 0; c < 2; ++c) {
      f32x4 s = *reinterpret_cast<const f32x4*>(P + 0 * (64 * 68) + m * 68 + nb + c * 4);
#pragma unroll
      for (int w = 1; w < 4; ++w) s += *reinterpret_cast<const f32x4*>(P + w * (64 * 68) + m * 68 + nb + c * 4);
      ob[2 * c] = pk2bf(silu2(bfr2(f32x2{s[0], s[1]})));
      ob[2 * c + 1] = pk2bf(silu2(bfr2(f32x2{s[2], s[3]})));
    }
    *reinterpret_cast<u32x4*>(G + (long)(m0 + m) * DM + n0 + nb) = u32x4{ob[0], ob[1], ob[2], ob[3]};
  }
  STAMPK(2, 4);
}

__global__ __launch_bounds__(256) void k3_irfft_combine(const float2* __restrict__ Z2T, const uint16_t* __restrict__ x,
                                                        const uint16_t* __restrict__ G, uint16_t* __restrict__ out) {
  __shared__ cf zs[8 * ZRS];
  const int tid = threadIdx.x, s0 = blockIdx.x * 8;
  const int r = tid >> 5, t = tid & 31;
  const int r2 = tid & 7, kb = tid >> 3;
  const int row = s0 + r;
  float2 y0[4], y1[4], y128;
#pragma unroll
  for (int it = 0; it < 4; ++it) {
    const int k = kb + 32 * it;
    y0[it] = Z2T[k * SEQ + s0 + r2];
    y1[it] = Z2T[(256 - k) * SEQ + s0 + r2];
  }
  y128 = Z2T[128 * SEQ + s0 + r2];
  cf wsp[4];
#pragma unroll
  for (int it = 0; it < 4; ++it) wsp[it] = tw(4 * (kb + 32 * it));
  const cf wp2 = tw(32 * (t & 7));
  const cf wp3[2] = {tw(8 * t), tw(8 * (t + 32))};
  const uint32_t* xrow = reinterpret_cast<const uint32_t*>(x + row * DM);
  const uint32_t* grow = reinterpret_cast<const uint32_t*>(G + row * DM);
  uint32_t xu[2][4], gu[2][4];
#pragma unroll
  for (int h = 0; h < 2; ++h)
#pragma unroll
    for (int j = 0; j < 4; ++j) { xu[h][j] = xrow[t + 32 * h + 64 * j]; gu[h][j] = grow[t + 32 * h + 64 * j]; }
  {
    cf* zr = zs + r2 * ZRS;
#pragma unroll
    for (int it = 0; it < 4; ++it) {
      const int k = kb + 32 * it;
      float2 a0 = y0[it], a1 = y1[it];
      if (k == 0) { a0.y = 0.f; a1.y = 0.f; }
      const cf E = cf{0.5f * (a0.x + a1.x), 0.5f * (a0.y - a1.y)};
      const cf Dd = cf{0.5f * (a0.x - a1.x), 0.5f * (a0.y + a1.y)};
      const cf O = cmulc(Dd, wsp[it]);
      zr[swz(k)] = cf{E.x - O.y, E.y + O.x};
      if (k != 0) zr[swz(256 - k)] = cf{E.x + O.y, -E.y + O.x};
    }
    if (kb == 0) zr[swz(128)] = cf{y128.x, -y128.y};
  }
  __syncthreads();
  cf* zrow = zs + r * ZRS;
  cf a[8];
#pragma unroll
  for (int j = 0; j < 8; ++j) a[j] = zrow[swz(t + 32 * j)];
  wsync();
  cf res[2][4];
  fft256_row<1>(a, res, zrow, t, wp2, wp3);
  uint32_t* orow = reinterpret_cast<uint32_t*>(out + row * DM);
#pragma unroll
  for (int h = 0; h < 2; ++h)
#pragma unroll
    for (int j = 0; j < 4; ++j) {
      const int n = t + 32 * h + 64 * j;
      const float s_0 = bfr(res[h][j].x * (1.0f / 256.0f)), s_1 = bfr(res[h][j].y * (1.0f / 256.0f));
      const float o0 = bf2f(xu[h][j] & 0xffffu) + bfr(s_0 * bf2f(gu[h][j] & 0xffffu));
      const float o1 = bf2f(xu[h][j] >> 16) + bfr(s_1 * bf2f(gu[h][j] >> 16));
      orow[n] = f2bf(o0) | (f2bf(o1) << 16);
    }
}

}

extern "C" void kernel_launch(void* const* d_in, const int* in_sizes, int n_in, void* d_out, int out_size, void* d_ws,
                              size_t ws_size, hipStream_t stream) {
  const uint16_t* x  = (const uint16_t*)d_in[0];
  const uint16_t* wr = (const uint16_t*)d_in[1];
  const uint16_t* wi = (const uint16_t*)d_in[2];
  const uint16_t* wg = (const uint16_t*)d_in[4];
  const uint16_t* wu = (const uint16_t*)d_in[5];
  const uint16_t* wd = (const uint16_t*)d_in[6];
  uint16_t* out = (uint16_t*)d_out;
  char* ws = (char*)d_ws;
  uint16_t* xn = (uint16_t*)(ws);
  float2* X1T = (float2*)(ws + (4u << 20));
  uint32_t* WT = (uint32_t*)(ws + (12u << 20));
  float2* Z2T = (float2*)(ws + (16u << 20));
  uint16_t* H = (uint16_t*)(ws + (24u << 20));
  uint16_t* G = (uint16_t*)(ws + (32u << 20));

  (void)hipFuncSetAttribute((const void*)g1k2, hipFuncAttributeMaxDynamicSharedMemorySize, 3 * G1_STAGE);
  (void)hipFuncSetAttribute((const void*)g2_gemm, hipFuncAttributeMaxDynamicSharedMemorySize, G2_LDS);
  constexpr int FFT_PAD = 64 * 1024;
  (void)hipFuncSetAttribute((const void*)k1_ln_rfft, hipFuncAttributeMaxDynamicSharedMemorySize, FFT_PAD);
  (void)hipFuncSetAttribute((const void*)k3_irfft_combine, hipFuncAttributeMaxDynamicSharedMemorySize, FFT_PAD);

  k1_ln_rfft<<<256, 256, FFT_PAD, stream>>>(x, wr, wi, xn, X1T, WT, wg, wu, wd);
  g1k2<<<224 + 256, 512, 3 * G1_STAGE, stream>>>(xn, wg, wu, H, X1T, WT, Z2T);
  g2_gemm<<<256, 512, G2_LDS, stream>>>(H, wd, G);
  k3_irfft_combine<<<256, 256, FFT_PAD, stream>>>(Z2T, x, G, out);
}
```
